```python
import torch

def _softmax(x, axis=-1):
    return torch.softmax(x, dim=axis)
def _gelu(x):
    return torch.nn.functional.gelu(x, approximate="tanh")
def _silu(x):
    return torch.nn.functional.silu(x)
def _sigmoid(x):
    return torch.sigmoid(x)
def _layernorm(x, axis=-1, eps=1e-5):
    m = x.mean(dim=axis, keepdim=True)
    v = x.var(dim=axis, keepdim=True, unbiased=False)
    return (x - m) / torch.sqrt(v + eps)
def _scatter_add(tgt, idx, src, axis=0):
    return tgt.clone().index_add_(axis, idx, src)
def _segment_sum(src, seg, n):
    out = torch.zeros((n,) + src.shape[1:], dtype=src.dtype)
    return out.index_add_(0, seg, src)
def _pad(x, pw):
    flat = tuple(v for pair in reversed(pw) for v in pair)
    return torch.nn.functional.pad(x, flat)

kernel_name = 'concat_heads__gqa_block_cached__gqa_block_cached__decode'

def setup_inputs():

    _w = (lambda a: (lambda t: t.to(torch.bfloat16) if t.is_floating_point() else t)(torch.as_tensor(a)))
    def _A_setup():
        B, Sq, D = 2, 1, 1024
        Hq, Hkv, Sk, Dh = 32, 4, 32768, 128
        assert Hq % Hkv == 0, (Hq, Hkv)
        return {
            "x": _w((lambda *s: torch.randn(*s, dtype=torch.bfloat16) * 0.8)(B, Sq, D)),
            "k_cache": _w((lambda *s: torch.randn(*s, dtype=torch.bfloat16) * 0.8)(B, Hkv, Sk, Dh)),
            "v_cache": _w((lambda *s: torch.randn(*s, dtype=torch.bfloat16) * 0.05)(B, Hkv, Sk, Dh)),
            "wq": _w((lambda *s: torch.randn(*s, dtype=torch.bfloat16) * 0.8)(D, Hq * Dh) * (D ** -0.5)),
            "wo": _w((lambda *s: torch.randn(*s, dtype=torch.bfloat16) * 0.05)(Hq * Dh, D) * ((Hq * Dh) ** -0.5)),
        }
    _A_in = _A_setup()

    def _B_setup():
        B, Sq, D = 2, 1, 1024
        Hq, Hkv, Sk, Dh = 32, 4, 32768, 128
        assert Hq % Hkv == 0, (Hq, Hkv)
        return {
            "x": _w((lambda *s: torch.randn(*s, dtype=torch.bfloat16) * 0.8)(B, Sq, D)),
            "k_cache": _w((lambda *s: torch.randn(*s, dtype=torch.bfloat16) * 0.8)(B, Hkv, Sk, Dh)),
            "v_cache": _w((lambda *s: torch.randn(*s, dtype=torch.bfloat16) * 0.05)(B, Hkv, Sk, Dh)),
            "wq": _w((lambda *s: torch.randn(*s, dtype=torch.bfloat16) * 0.8)(D, Hq * Dh) * (D ** -0.5)),
            "wo": _w((lambda *s: torch.randn(*s, dtype=torch.bfloat16) * 0.05)(Hq * Dh, D) * ((Hq * Dh) ** -0.5)),
        }
    _B_in = _B_setup()

    return {
        "A_x": _A_in["x"],
        "A_k_cache": _A_in["k_cache"],
        "A_v_cache": _A_in["v_cache"],
        "A_wq": _A_in["wq"],
        "A_wo": _A_in["wo"],
        "B_x": _B_in["x"],
        "B_k_cache": _B_in["k_cache"],
        "B_v_cache": _B_in["v_cache"],
        "B_wq": _B_in["wq"],
        "B_wo": _B_in["wo"],
    }

def reference(A_x, A_k_cache, A_v_cache, A_wq, A_wo, B_x, B_k_cache, B_v_cache, B_wq, B_wo):
    def _A_ref(x, k_cache, v_cache, wq, wo):
        B, Sq, D = x.shape
        Hq, Dh = 32, 128
        Hkv = k_cache.shape[1]
        rep = Hq // Hkv
        q = (lambda x, *d: x.permute(*d))((x @ wq).reshape(B, Sq, Hq, Dh), 0, 2, 1, 3)
        k = (lambda x, n, axis: torch.repeat_interleave(x, n, dim=axis))(k_cache, rep, axis=1)
        v = (lambda x, n, axis: torch.repeat_interleave(x, n, dim=axis))(v_cache, rep, axis=1)
        scores = torch.einsum("bhqd,bhkd->bhqk", q, k) * (Dh ** -0.5)
        p = _softmax(scores, axis=-1)
        o = torch.einsum("bhqk,bhkd->bhqd", p, v)
        return (lambda x, *d: x.permute(*d))(o, 0, 2, 1, 3).reshape(B, Sq, Hq * Dh) @ wo

    def _B_ref(x, k_cache, v_cache, wq, wo):
        B, Sq, D = x.shape
        Hq, Dh = 32, 128
        Hkv = k_cache.shape[1]
        rep = Hq // Hkv
        q = (lambda x, *d: x.permute(*d))((x @ wq).reshape(B, Sq, Hq, Dh), 0, 2, 1, 3)
        k = (lambda x, n, axis: torch.repeat_interleave(x, n, dim=axis))(k_cache, rep, axis=1)
        v = (lambda x, n, axis: torch.repeat_interleave(x, n, dim=axis))(v_cache, rep, axis=1)
        scores = torch.einsum("bhqd,bhkd->bhqk", q, k) * (Dh ** -0.5)
        p = _softmax(scores, axis=-1)
        o = torch.einsum("bhqk,bhkd->bhqd", p, v)
        return (lambda x, *d: x.permute(*d))(o, 0, 2, 1, 3).reshape(B, Sq, Hq * Dh) @ wo

    a = _A_ref(A_x, A_k_cache, A_v_cache, A_wq, A_wo)
    b = _B_ref(B_x, B_k_cache, B_v_cache, B_wq, B_wo)
    return torch.concatenate([a, b], axis=-1)
```

```cpp
#include <hip/hip_runtime.h>
#include <hip/hip_bf16.h>
#include <stdint.h>

typedef unsigned v4u __attribute__((ext_vector_type(4)));
typedef unsigned v2u __attribute__((ext_vector_type(2)));
typedef float f32x4 __attribute__((ext_vector_type(4)));
typedef __bf16 bf16x8 __attribute__((ext_vector_type(8)));
typedef __bf16 bf16x2 __attribute__((ext_vector_type(2)));
typedef unsigned long long u64;
typedef __amdgpu_buffer_rsrc_t rsrc_t;

namespace {

constexpr int kD = 1024, kHq = 32, kHkv = 4, kSk = 32768, kDh = 128;
constexpr int kNWG = 256, kNThr = 512, kNTile = 8;
constexpr int kKeysPerWG = 2048;
constexpr int AUX_NT = 2, AUX_SC1 = 16;
constexpr int RSRC_W3 = 0x00020000;
constexpr unsigned kSent = 0xFFFFFFFFu;
constexpr unsigned kSpinCap = 1u << 18;
constexpr float kC2 = 0.088388347648318440f * 1.4426950408889634f;

constexpr int WS_EPOCH = 0;
constexpr int WS_QG = 4096;
constexpr int WS_ML = WS_QG + 8 * 2 * 1024 * 8;
constexpr int WS_PART = WS_ML + 8 * 8 * 32 * 8;
constexpr int WS_OP = WS_PART + 8 * 32 * 4096;
constexpr int WS_END = WS_OP + 2 * 2 * 64 * 4096;

constexpr int L_QRED = 0;
constexpr int L_QIMG = L_QRED + 8 * 128 * 4;
constexpr int L_MRG = L_QIMG + 2048;
constexpr int L_MRGML = L_MRG + 8 * 8 * 132 * 4;
constexpr int L_ORED = L_MRGML + 8 * 16 * 4;
constexpr int L_END = L_ORED + 8 * 1024 * 4;

struct Params {
  const unsigned short* x[2];
  const unsigned short* k[2];
  const unsigned short* v[2];
  const unsigned short* wq[2];
  const unsigned short* wo[2];
  unsigned short* out;
  unsigned char* ws;
};

__device__ __forceinline__ rsrc_t make_rsrc(const void* p, int bytes) {
  return __builtin_amdgcn_make_buffer_rsrc((void*)p, 0, bytes, RSRC_W3);
}
template <int AUX> __device__ __forceinline__ v4u ld16(rsrc_t r, int voff, int soff) {
  return __builtin_amdgcn_raw_buffer_load_b128(r, voff, soff, AUX);
}
__device__ __forceinline__ f32x4 mfma16(v4u a, v4u b, f32x4 c) {
  return __builtin_amdgcn_mfma_f32_16x16x32_bf16(__builtin_bit_cast(bf16x8, a), __builtin_bit_cast(bf16x8, b), c, 0, 0, 0);
}
__device__ __forceinline__ float dot2(unsigned a, unsigned b, float c) {
  return __builtin_amdgcn_fdot2_f32_bf16(__builtin_bit_cast(bf16x2, a), __builtin_bit_cast(bf16x2, b), c, false);
}
__device__ __forceinline__ unsigned pack_bf16(float lo, float hi) {
  bf16x2 r = {(__bf16)lo, (__bf16)hi};
  return __builtin_bit_cast(unsigned, r);
}
template <int CTRL> __device__ __forceinline__ float dpp(float x) {
  return __builtin_bit_cast(float, __builtin_amdgcn_mov_dpp(__builtin_bit_cast(int, x), CTRL, 0xf, 0xf, true));
}
constexpr int ROR2 = 0x122, ROR4 = 0x124, ROR8 = 0x128;
__device__ __forceinline__ float swap16_add(float a, float b) {
  auto s = __builtin_amdgcn_permlane16_swap(__float_as_uint(a), __float_as_uint(b), false, false);
  return __uint_as_float(s[0]) + __uint_as_float(s[1]);
}
__device__ __forceinline__ float swap32_add(float a, float b) {
  auto s = __builtin_amdgcn_permlane32_swap(__float_as_uint(a), __float_as_uint(b), false, false);
  return __uint_as_float(s[0]) + __uint_as_float(s[1]);
}
__device__ __forceinline__ float swap16_max(float a) {
  auto s = __builtin_amdgcn_permlane16_swap(__float_as_uint(a), __float_as_uint(a), false, false);
  return fmaxf(__uint_as_float(s[0]), __uint_as_float(s[1]));
}
__device__ __forceinline__ float swap32_max(float a) {
  auto s = __builtin_amdgcn_permlane32_swap(__float_as_uint(a), __float_as_uint(a), false, false);
  return fmaxf(__uint_as_float(s[0]), __uint_as_float(s[1]));
}
__device__ __forceinline__ unsigned perm_lo(unsigned hi, unsigned lo) { return __builtin_amdgcn_perm(hi, lo, 0x05040100u); }
__device__ __forceinline__ unsigned perm_hi(unsigned hi, unsigned lo) { return __builtin_amdgcn_perm(hi, lo, 0x07060302u); }
__device__ __forceinline__ v4u column_of(const v4u (&r)[8], int t) {
  v4u o;
#pragma unroll
  for (int u = 0; u < 4; ++u)
    o[u] = (t & 1) ? perm_hi(r[2 * u + 1][t >> 1], r[2 * u][t >> 1]) : perm_lo(r[2 * u + 1][t >> 1], r[2 * u][t >> 1]);
  return o;
}
__device__ __forceinline__ unsigned not_sentinel(float f) {
  const unsigned u = __float_as_uint(f);
  return u == kSent ? 0x7FC00000u : u;
}
__device__ __forceinline__ bool valid4(v4u a) { return a[0] != kSent && a[1] != kSent && a[2] != kSent && a[3] != kSent; }
__device__ __forceinline__ u64 mix64(u64 z) {
  z += 0x9E3779B97F4A7C15ull;
  z = (z ^ (z >> 30)) * 0xBF58476D1CE4E5B9ull;
  z = (z ^ (z >> 27)) * 0x94D049BB133111EBull;
  return z ^ (z >> 31);
}
#define PIN() __builtin_amdgcn_sched_barrier(0)
#define MEMFENCE() asm volatile("" ::: "memory")
__device__ __forceinline__ void wg_barrier() {
  asm volatile("s_waitcnt lgkmcnt(0)" ::: "memory");
  __builtin_amdgcn_s_barrier();
  asm volatile("" ::: "memory");
}

__global__ void __launch_bounds__(kNThr, 2) fused_decode(Params p) {
  __shared__ __attribute__((aligned(16))) unsigned char lds[L_END];
  const int tid = threadIdx.x, lane = tid & 63;
  const int wave = __builtin_amdgcn_readfirstlane(tid >> 6);
  const int wg = blockIdx.x, pair = wg & 7, idx = wg >> 3;
  const int br = pair >> 2, kvh = pair & 3, b = idx >> 4, split = idx & 15;
  const int hl = idx >> 2, rh = (idx >> 1) & 1, cb = idx & 1;

  const rsrc_t rws = make_rsrc(p.ws, WS_END);
  const size_t kv_off = ((size_t)(b * kHkv + kvh) * kSk + (size_t)split * kKeysPerWG) * kDh;
  const rsrc_t rk = make_rsrc(p.k[br] + kv_off, kKeysPerWG * kDh * 2);
  const rsrc_t rv = make_rsrc(p.v[br] + kv_off, kKeysPerWG * kDh * 2);
  const rsrc_t rq = make_rsrc(p.wq[br] + kvh * 1024 + idx * 32, (kD - 1) * kHq * kDh * 2 + 64);
  const rsrc_t rx = make_rsrc(p.x[br], 2 * kD * 2);
  const rsrc_t ro = make_rsrc(p.wo[br] + (size_t)((kvh * 8 + hl) * kDh + rh * 64) * kD + cb * 512, 63 * kD * 2 + 1024);

  const v2u ep = __builtin_amdgcn_raw_buffer_load_b64(rws, 0, WS_EPOCH + br * 256, AUX_SC1);
  const int op_slot = WS_OP + ((br * 2 + cb) * 64 + (kvh * 16 + (idx >> 1))) * 4096;
  {
    const v4u s4 = {kSent, kSent, kSent, kSent};
    const int off = tid < 256 ? WS_PART + (pair * 32 + idx) * 4096 + tid * 16 : op_slot + (tid - 256) * 16;
    __builtin_amdgcn_raw_buffer_store_b128(s4, rws, off, 0, AUX_SC1);
    if (lane == 0) {
      const v2u s2 = {kSent, kSent};
      __builtin_amdgcn_raw_buffer_store_b64(s2, rws, WS_ML + ((pair * 8 + wave) * 32 + idx) * 8, 0, AUX_SC1);
    }
  }
  PIN();
  v4u xw[2], wqr[8];
  {
    const int xo = (128 * wave + 8 * (lane >> 2)) * 2;
    xw[0] = ld16<0>(rx, xo, 0);
    xw[1] = ld16<0>(rx, xo, kD * 2);
    const int qo = (lane >> 2) * 8 * 8192 + (lane & 3) * 16;
#pragma unroll
    for (int i = 0; i < 8; ++i) wqr[i] = ld16<0>(rq, qo, (128 * wave + i) * 8192);
  }
  PIN();
  const int kl = (lane & 15) >> 1;
  int koff = (4 * (kl & 1) + (kl >> 1)) * 256 + (4 * (lane & 1) + (lane >> 4)) * 16;
  int voff = lane * 16;
  v4u Kt[2][8], Vt[2][8];
#define LOAD_K(buf, t)                                                                                               \
  do {                                                                                                               \
    asm volatile("" : "+v"(koff));                                                                                   \
    _Pragma("unroll") for (int i = 0; i < 8; ++i)                                                                    \
        Kt[buf][i] = ld16<AUX_NT>(rk, koff + (i & 2) * 1024 + (i & 1) * 128, ((t) * 8 + wave) * 8192 + (i >> 2) * 4096); \
  } while (0)
#define LOAD_V(buf, t)                                                                                               \
  do {                                                                                                               \
    asm volatile("" : "+v"(voff));                                                                                   \
    _Pragma("unroll") for (int i = 0; i < 8; ++i)                                                                    \
        Vt[buf][i] = ld16<AUX_NT>(rv, voff + (i & 3) * 1024, ((t) * 8 + wave) * 8192 + (i >> 2) * 4096);             \
  } while (0)
  LOAD_K(0, 0);
  PIN();
  LOAD_V(0, 0);
  PIN();

  {
    float qa[16];
#pragma unroll
    for (int e = 0; e < 8; ++e) {
      const v4u c = column_of(wqr, e);
#pragma unroll
      for (int bb = 0; bb < 2; ++bb) {
        float a = 0.f;
#pragma unroll
        for (int u = 0; u < 4; ++u) a = dot2(c[u], xw[bb][u], a);
        qa[bb * 8 + e] = a;
      }
    }
    float r8[8], r4[4];
#pragma unroll
    for (int e = 0; e < 8; ++e) r8[e] = swap32_add(qa[e], qa[8 + e]);
#pragma unroll
    for (int e = 0; e < 4; ++e) r4[e] = swap16_add(r8[e], r8[4 + e]);
    const bool up = lane & 8;
    const float o0 = (up ? r4[2] : r4[0]) + dpp<ROR8>(up ? r4[0] : r4[2]);
    const float o1 = (up ? r4[3] : r4[1]) + dpp<ROR8>(up ? r4[1] : r4[3]);
    asm volatile("s_waitcnt vmcnt(16)" ::: "memory");
    *reinterpret_cast<float2*>(lds + L_QRED + wave * 512 + lane * 8) = make_float2(o0, o1);
  }
  wg_barrier();
  const u64 epoch = ((u64)(unsigned)__builtin_amdgcn_readfirstlane(ep[1]) << 32) | (unsigned)__builtin_amdgcn_readfirstlane(ep[0]);
  const u64 tag = mix64(epoch) & ~0xFFFFull;
  const unsigned tag_lo = (unsigned)tag, tag_hi = (unsigned)(tag >> 32);
  if (wave == 0) {
    const int bb = lane >> 5, col = lane & 31;
    const int src = bb * 32 + ((col >> 2) & 1) * 16 + ((col >> 1) & 1) * 8 + (col >> 3);
    float s = 0.f;
#pragma unroll
    for (int w = 0; w < 8; ++w) {
      const float* q = reinterpret_cast<const float*>(lds + L_QRED + w * 512);
      s += q[src * 2 + (col & 1)] + q[(src + 4) * 2 + (col & 1)];
    }
    const v2u g = {tag_lo | (pack_bf16(s, 0.f) & 0xFFFFu), tag_hi};
    __builtin_amdgcn_raw_buffer_store_b64(g, rws, WS_QG + ((pair * 2 + bb) * 1024 + idx * 32 + col) * 8, 0, AUX_SC1);
  }
  PIN();
  asm volatile("s_waitcnt vmcnt(9)" ::: "memory");
  const int qg_off = WS_QG + (pair * 2 + b) * 8192 + tid * 16;
  v4u g = ld16<AUX_SC1>(rws, qg_off, 0);
  PIN();
  LOAD_K(1, 1);
  PIN();
  LOAD_V(1, 1);
  PIN();
#define Q_OK(g) (((g[0] ^ tag_lo) >> 16) == 0u && g[1] == tag_hi && ((g[2] ^ tag_lo) >> 16) == 0u && g[3] == tag_hi)
  if (!__all(Q_OK(g))) {
    unsigned spins = 0;
    do {
      __builtin_amdgcn_s_sleep(2);
      MEMFENCE();
      g = ld16<AUX_SC1>(rws, qg_off, 0);
    } while (!__all(Q_OK(g)) && ++spins < kSpinCap);
  }
#undef Q_OK
  *reinterpret_cast<unsigned*>(lds + L_QIMG + tid * 4) = perm_lo(g[2], g[0]);
  wg_barrier();
  const int pp = (lane >> 3) & 1, hh = lane & 7, gg = lane >> 4;
  const v4u Q0 = *reinterpret_cast<const v4u*>(lds + L_QIMG + hh * 256 + pp * 64 + gg * 16);
  const v4u Q1 = *reinterpret_cast<const v4u*>(lds + L_QIMG + hh * 256 + 128 + pp * 64 + gg * 16);

  f32x4 acc[8];
#pragma unroll
  for (int t = 0; t < 8; ++t) acc[t] = f32x4{0.f, 0.f, 0.f, 0.f};
  float m = -INFINITY, l = 0.f;
  const unsigned pmask = pp ? 0xFFFF0000u : 0x0000FFFFu;
  v4u wor[8];
#pragma unroll
  for (int t = 0; t < kNTile; ++t) {
    const int buf = t & 1;
    float s[4];
#pragma unroll
    for (int T = 0; T < 4; ++T) {
      f32x4 d = mfma16(Kt[buf][2 * T], Q0, f32x4{0.f, 0.f, 0.f, 0.f});
      d = mfma16(Kt[buf][2 * T + 1], Q1, d);
      s[T] = (pp ? d[3] : d[0]) + dpp<ROR8>(pp ? d[1] : d[2]);
    }
    PIN();
    if (t + 2 < kNTile) {
      LOAD_K(buf, t + 2);
    } else if (t + 2 == kNTile) {
#pragma unroll
      for (int i = 0; i < 8; ++i) wor[i] = ld16<AUX_NT>(ro, lane * 16, (8 * wave + i) * 2048);
    }
    PIN();
    float mt = fmaxf(fmaxf(s[0], s[1]), fmaxf(s[2], s[3]));
    mt = fmaxf(mt, dpp<ROR8>(mt));
    mt = swap32_max(swap16_max(mt)) * kC2;
    const float mn = fmaxf(m, mt);
    if (__any(mn != m)) {
      const float alpha = __builtin_amdgcn_exp2f(m - mn);
      l *= alpha;
#pragma unroll
      for (int i = 0; i < 8; ++i) acc[i] *= alpha;
      m = mn;
    }
    v4u P;
#pragma unroll
    for (int T = 0; T < 4; ++T) {
      const float e = __builtin_amdgcn_exp2f(fmaf(s[T], kC2, -m));
      l += e;
      P[T] = pack_bf16(e, e) & pmask;
    }
#pragma unroll
    for (int i = 0; i < 8; ++i) acc[i] = mfma16(column_of(Vt[buf], i), P, acc[i]);
    PIN();
    if (t + 2 < kNTile) {
      LOAD_V(buf, t + 2);
    }
    PIN();
  }
#undef LOAD_K
#undef LOAD_V

  l += dpp<ROR8>(l);
  l = swap16_add(l, l);
  l = swap32_add(l, l);
  {
    float* dst = reinterpret_cast<float*>(lds + L_MRG) + (wave * 8 + hh) * 132 + 32 * gg + 16 * pp;
#pragma unroll
    for (int rr = 0; rr < 2; ++rr)
#pragma unroll
      for (int q = 0; q < 2; ++q) {
        f32x4 o;
#pragma unroll
        for (int e = 0; e < 4; ++e) {
          const f32x4 a = acc[4 * q + e];
          o[e] = (pp ? a[2 + rr] : a[rr]) + dpp<ROR8>(pp ? a[rr] : a[2 + rr]);
        }
        *reinterpret_cast<f32x4*>(dst + 8 * rr + 4 * q) = o;
      }
    if (lane < 8) {
      float* ml = reinterpret_cast<float*>(lds + L_MRGML) + lane * 16;
      ml[wave] = m;
      ml[8 + wave] = l;
    }
  }
  wg_barrier();
  {
    const float* ml = reinterpret_cast<const float*>(lds + L_MRGML) + wave * 16;
    const f32x4 m0 = *reinterpret_cast<const f32x4*>(ml), m1 = *reinterpret_cast<const f32x4*>(ml + 4);
    const f32x4 l0 = *reinterpret_cast<const f32x4*>(ml + 8), l1 = *reinterpret_cast<const f32x4*>(ml + 12);
    const float M = fmaxf(fmaxf(fmaxf(m0[0], m0[1]), fmaxf(m0[2], m0[3])), fmaxf(fmaxf(m1[0], m1[1]), fmaxf(m1[2], m1[3])));
    float L = 0.f, o0 = 0.f, o1 = 0.f;
#pragma unroll
    for (int w = 0; w < 8; ++w) {
      const float e = __builtin_amdgcn_exp2f((w < 4 ? m0[w & 3] : m1[w & 3]) - M);
      const float2 o = *reinterpret_cast<const float2*>(lds + L_MRG + ((w * 8 + wave) * 132 + 2 * lane) * 4);
      L = fmaf(e, w < 4 ? l0[w & 3] : l1[w & 3], L);
      o0 = fmaf(e, o.x, o0);
      o1 = fmaf(e, o.y, o1);
    }
    const v2u ov = {not_sentinel(o0), not_sentinel(o1)};
    __builtin_amdgcn_raw_buffer_store_b64(ov, rws, WS_PART + (pair * 32 + idx) * 4096 + wave * 512 + lane * 8, 0, AUX_SC1);
    if (lane == 0) {
      const v2u mv = {not_sentinel(M), not_sentinel(L)};
      __builtin_amdgcn_raw_buffer_store_b64(mv, rws, WS_ML + ((pair * 8 + wave) * 32 + idx) * 8, 0, AUX_SC1);
    }
  }
  PIN();

  v4u wc[8];
#pragma unroll
  for (int e = 0; e < 8; ++e) wc[e] = column_of(wor, e);
  PIN();
  v2u chk = {tag_lo, tag_hi};
  const int chk_off = WS_QG + ((br * 4 + (lane >> 4)) * 2 * 1024 + (2 * (lane & 15) + ((wg >> 3) & 1)) * 32) * 8;
  if (wave == 0) chk = __builtin_amdgcn_raw_buffer_load_b64(rws, chk_off, 0, AUX_SC1);
  const int po = WS_PART + (pair * 32 + (lane >> 1)) * 4096 + (hl * 128 + rh * 64 + 8 * wave + 4 * (lane & 1)) * 4;
  const int mo = WS_ML + ((pair * 8 + hl) * 32 + (lane >> 1)) * 8;
  v4u pd = ld16<AUX_SC1>(rws, po, 0);
  v2u pm = __builtin_amdgcn_raw_buffer_load_b64(rws, mo, 0, AUX_SC1);
  for (unsigned spins = 0;; ++spins) {
    const bool ok = valid4(pd) && pm[0] != kSent && pm[1] != kSent;
    if (__all(ok) || spins > kSpinCap) break;
    __builtin_amdgcn_s_sleep(1);
    MEMFENCE();
    if (!ok) {
      pd = ld16<AUX_SC1>(rws, po, 0);
      pm = __builtin_amdgcn_raw_buffer_load_b64(rws, mo, 0, AUX_SC1);
    }
  }
  {
    const float ms = __uint_as_float(pm[0]), ls = __uint_as_float(pm[1]);
    float M = fmaxf(ms, dpp<ROR2>(ms));
    M = fmaxf(M, dpp<ROR4>(M));
    M = fmaxf(M, dpp<ROR8>(M));
    M = swap16_max(M);
    const float e = __builtin_amdgcn_exp2f(ms - M);
    float v[5] = {e * ls, e * __uint_as_float(pd[0]), e * __uint_as_float(pd[1]), e * __uint_as_float(pd[2]), e * __uint_as_float(pd[3])};
#pragma unroll
    for (int i = 0; i < 5; ++i) {
      float y = v[i];
      y += dpp<ROR2>(y);
      y += dpp<ROR4>(y);
      y += dpp<ROR8>(y);
      v[i] = swap16_add(y, y);
    }
    const float inv = 1.f / v[0];
    const unsigned o01 = pack_bf16(v[1] * inv, v[2] * inv), o23 = pack_bf16(v[3] * inv, v[4] * inv);
    float oa[2][8];
#pragma unroll
    for (int bb = 0; bb < 2; ++bb) {
      const unsigned s0 = __builtin_amdgcn_readlane(o01, 32 * bb), s1 = __builtin_amdgcn_readlane(o23, 32 * bb);
      const unsigned s2 = __builtin_amdgcn_readlane(o01, 32 * bb + 1), s3 = __builtin_amdgcn_readlane(o23, 32 * bb + 1);
#pragma unroll
      for (int e2 = 0; e2 < 8; ++e2)
        oa[bb][e2] = dot2(wc[e2][3], s3, dot2(wc[e2][2], s2, dot2(wc[e2][1], s1, dot2(wc[e2][0], s0, 0.f))));
    }
    float* dst = reinterpret_cast<float*>(lds + L_ORED) + wave * 1024 + lane * 4;
#pragma unroll
    for (int bb = 0; bb < 2; ++bb)
#pragma unroll
      for (int q = 0; q < 2; ++q)
        *reinterpret_cast<f32x4*>(dst + bb * 512 + q * 256) = f32x4{oa[bb][4 * q], oa[bb][4 * q + 1], oa[bb][4 * q + 2], oa[bb][4 * q + 3]};
  }
  wg_barrier();
  {
    float o0 = 0.f, o1 = 0.f;
#pragma unroll
    for (int w = 0; w < 8; ++w) {
      const float2 o = *reinterpret_cast<const float2*>(lds + L_ORED + w * 4096 + tid * 8);
      o0 += o.x;
      o1 += o.y;
    }
    const int bb = tid >> 8, q = (tid >> 7) & 1, ln = (tid >> 1) & 63, e = (tid & 1) * 2;
    const v2u ov = {not_sentinel(o0), not_sentinel(o1)};
    __builtin_amdgcn_raw_buffer_store_b64(ov, rws, op_slot + (bb * 512 + 8 * ln + 4 * q + e) * 4, 0, AUX_SC1);
  }
  if (wave != 0) return;

  for (unsigned spins = 0;; ++spins) {
    const bool ok = ((chk[0] ^ tag_lo) >> 16) == 0u && chk[1] == tag_hi;
    if (__all(ok) || spins > kSpinCap) break;
    __builtin_amdgcn_s_sleep(2);
    MEMFENCE();
    chk = __builtin_amdgcn_raw_buffer_load_b64(rws, chk_off, 0, AUX_SC1);
  }
  MEMFENCE();
  {
    const int r = kvh * 16 + (idx >> 1);
    const int ob = r >> 5, cg = r & 31;
    const int fo = WS_OP + ((br * 2 + cb) * 64 + (lane >> 2)) * 4096 + (ob * 512 + cg * 16 + (lane & 3) * 4) * 4;
    v4u f[4];
#pragma unroll
    for (int q = 0; q < 4; ++q) f[q] = ld16<AUX_SC1>(rws, fo, q * 16 * 4096);
    for (unsigned spins = 0;; ++spins) {
      const bool ok = valid4(f[0]) && valid4(f[1]) && valid4(f[2]) && valid4(f[3]);
      if (__all(ok) || spins > kSpinCap) break;
      __builtin_amdgcn_s_sleep(1);
      MEMFENCE();
      if (!ok) {
#pragma unroll
        for (int q = 0; q < 4; ++q) f[q] = ld16<AUX_SC1>(rws, fo, q * 16 * 4096);
      }
    }
    float y[4];
#pragma unroll
    for (int e = 0; e < 4; ++e) {
      float s = (__uint_as_float(f[0][e]) + __uint_as_float(f[1][e])) + (__uint_as_float(f[2][e]) + __uint_as_float(f[3][e]));
      s += dpp<ROR4>(s);
      s += dpp<ROR8>(s);
      s = swap16_add(s, s);
      y[e] = swap32_add(s, s);
    }
    if (lane < 4) {
      const v2u o = {pack_bf16(y[0], y[1]), pack_bf16(y[2], y[3])};
      *reinterpret_cast<v2u*>(p.out + ob * 2048 + br * 1024 + cb * 512 + cg * 16 + lane * 4) = o;
    }
    if (kvh == 0 && idx == 0 && lane == 0) {
      const u64 nx = epoch + 1;
      const v2u e2 = {(unsigned)nx, (unsigned)(nx >> 32)};
      __builtin_amdgcn_raw_buffer_store_b64(e2, rws, WS_EPOCH + br * 256, 0, AUX_SC1);
    }
  }
}

}

extern "C" void kernel_launch(void* const* d_in, const int* in_sizes, int n_in, void* d_out, int out_size, void* d_ws,
                              size_t ws_size, hipStream_t stream) {
  (void)in_sizes; (void)n_in; (void)out_size; (void)ws_size;
  Params p;
  for (int i = 0; i < 2; ++i) {
    p.x[i] = (const unsigned short*)d_in[5 * i + 0];
    p.k[i] = (const unsigned short*)d_in[5 * i + 1];
    p.v[i] = (const unsigned short*)d_in[5 * i + 2];
    p.wq[i] = (const unsigned short*)d_in[5 * i + 3];
    p.wo[i] = (const unsigned short*)d_in[5 * i + 4];
  }
  p.out = (unsigned short*)d_out;
  p.ws = (unsigned char*)d_ws;
  hipLaunchKernelGGL(fused_decode, dim3(kNWG), dim3(kNThr), 0, stream, p);
}
```

```cpp
#include <hip/hip_runtime.h>
#include <hip/hip_bf16.h>
#include <stdint.h>

typedef unsigned v4u __attribute__((ext_vector_type(4)));
typedef unsigned v2u __attribute__((ext_vector_type(2)));
typedef float f32x4 __attribute__((ext_vector_type(4)));
typedef __bf16 bf16x8 __attribute__((ext_vector_type(8)));
typedef __bf16 bf16x2 __attribute__((ext_vector_type(2)));
typedef unsigned long long u64;
typedef __amdgpu_buffer_rsrc_t rsrc_t;

namespace {

constexpr int kD = 1024, kHq = 32, kHkv = 4, kSk = 32768, kDh = 128;
constexpr int kNWG = 256, kNThr = 512;
#define UNITS_EVEN 34
constexpr int kUnitsEven = UNITS_EVEN;
constexpr int AUX_NT = 2, AUX_SC1 = 16;
constexpr int RSRC_W3 = 0x00020000;
constexpr unsigned kSent = 0xFFFFFFFFu;
constexpr unsigned kSpinCap = 1u << 18;
constexpr float kC2 = 0.088388347648318440f * 1.4426950408889634f;

constexpr int WS_EPOCH = 0;
constexpr int WS_QG = 4096;
constexpr int WS_ML = WS_QG + 8 * 2 * 1024 * 8;
constexpr int WS_PART = WS_ML + 8 * 8 * 32 * 8;
constexpr int WS_OP = WS_PART + 8 * 32 * 4096;
constexpr int WS_END = WS_OP + 2 * 2 * 64 * 4096;

constexpr int L_QRED = 0;
constexpr int L_QIMG = L_QRED + 8 * 128 * 4;
constexpr int L_MRG = L_QIMG + 2048;
constexpr int L_MRGML = L_MRG + 8 * 8 * 132 * 4;
constexpr int L_ORED = L_MRGML + 8 * 16 * 4;
constexpr int L_END = L_ORED + 8 * 1024 * 4;

struct Params {
  const unsigned short* x[2];
  const unsigned short* k[2];
  const unsigned short* v[2];
  const unsigned short* wq[2];
  const unsigned short* wo[2];
  unsigned short* out;
  unsigned char* ws;
};

__device__ __forceinline__ rsrc_t make_rsrc(const void* p, int bytes) {
  return __builtin_amdgcn_make_buffer_rsrc((void*)p, 0, bytes, RSRC_W3);
}
template <int AUX> __device__ __forceinline__ v4u ld16(rsrc_t r, int voff, int soff) {
  return __builtin_amdgcn_raw_buffer_load_b128(r, voff, soff, AUX);
}
__device__ __forceinline__ f32x4 mfma16(v4u a, v4u b, f32x4 c) {
  return __builtin_amdgcn_mfma_f32_16x16x32_bf16(__builtin_bit_cast(bf16x8, a), __builtin_bit_cast(bf16x8, b), c, 0, 0, 0);
}
__device__ __forceinline__ float dot2(unsigned a, unsigned b, float c) {
  return __builtin_amdgcn_fdot2_f32_bf16(__builtin_bit_cast(bf16x2, a), __builtin_bit_cast(bf16x2, b), c, false);
}
__device__ __forceinline__ unsigned pack_bf16(float lo, float hi) {
  bf16x2 r = {(__bf16)lo, (__bf16)hi};
  return __builtin_bit_cast(unsigned, r);
}
template <int CTRL> __device__ __forceinline__ float dpp(float x) {
  return __builtin_bit_cast(float, __builtin_amdgcn_mov_dpp(__builtin_bit_cast(int, x), CTRL, 0xf, 0xf, true));
}
constexpr int ROR2 = 0x122, ROR4 = 0x124, ROR8 = 0x128;
__device__ __forceinline__ float swap16_add(float a, float b) {
  auto s = __builtin_amdgcn_permlane16_swap(__float_as_uint(a), __float_as_uint(b), false, false);
  return __uint_as_float(s[0]) + __uint_as_float(s[1]);
}
__device__ __forceinline__ float swap32_add(float a, float b) {
  auto s = __builtin_amdgcn_permlane32_swap(__float_as_uint(a), __float_as_uint(b), false, false);
  return __uint_as_float(s[0]) + __uint_as_float(s[1]);
}
__device__ __forceinline__ float swap16_max(float a) {
  auto s = __builtin_amdgcn_permlane16_swap(__float_as_uint(a), __float_as_uint(a), false, false);
  return fmaxf(__uint_as_float(s[0]), __uint_as_float(s[1]));
}
__device__ __forceinline__ float swap32_max(float a) {
  auto s = __builtin_amdgcn_permlane32_swap(__float_as_uint(a), __float_as_uint(a), false, false);
  return fmaxf(__uint_as_float(s[0]), __uint_as_float(s[1]));
}
__device__ __forceinline__ unsigned perm_lo(unsigned hi, unsigned lo) { return __builtin_amdgcn_perm(hi, lo, 0x05040100u); }
__device__ __forceinline__ unsigned perm_hi(unsigned hi, unsigned lo) { return __builtin_amdgcn_perm(hi, lo, 0x07060302u); }
__device__ __forceinline__ v4u column_of(const v4u (&r)[8], int t) {
  v4u o;
#pragma unroll
  for (int u = 0; u < 4; ++u)
    o[u] = (t & 1) ? perm_hi(r[2 * u + 1][t >> 1], r[2 * u][t >> 1]) : perm_lo(r[2 * u + 1][t >> 1], r[2 * u][t >> 1]);
  return o;
}
__device__ __forceinline__ unsigned not_sentinel(float f) {
  const unsigned u = __float_as_uint(f);
  return u == kSent ? 0x7FC00000u : u;
}
__device__ __forceinline__ bool valid4(v4u a) { return a[0] != kSent && a[1] != kSent && a[2] != kSent && a[3] != kSent; }
__device__ __forceinline__ u64 mix64(u64 z) {
  z += 0x9E3779B97F4A7C15ull;
  z = (z ^ (z >> 30)) * 0xBF58476D1CE4E5B9ull;
  z = (z ^ (z >> 27)) * 0x94D049BB133111EBull;
  return z ^ (z >> 31);
}
#define STAMP(i) do {} while (0)
#define STAMP_FLUSH() do {} while (0)
#define PIN() __builtin_amdgcn_sched_barrier(0)
#define MEMFENCE() asm volatile("" ::: "memory")
__device__ __forceinline__ void wg_barrier() {
  asm volatile("s_waitcnt lgkmcnt(0)" ::: "memory");
  __builtin_amdgcn_s_barrier();
  asm volatile("" ::: "memory");
}

__global__ void __launch_bounds__(kNThr, 2) fused_decode(Params p) {
  __shared__ __attribute__((aligned(16))) unsigned char lds[L_END];
  const int tid = threadIdx.x, lane = tid & 63;
  const int wave = __builtin_amdgcn_readfirstlane(tid >> 6);
  const int wg = blockIdx.x, pair = wg >> 5, idx = wg & 31;
  const int br = pair >> 2, kvh = pair & 3, b = idx >> 4, split = idx & 15;
  const int qblk = ((idx & 7) * 2 + ((idx >> 3) & 1)) + (idx & 16);
  const int hl = idx >> 2, rh = (idx >> 1) & 1, cb = idx & 1;

  STAMP(0);
  const rsrc_t rws = make_rsrc(p.ws, WS_END);
  const int nunit_wg = (split & 1) ? 64 - kUnitsEven : kUnitsEven;
  const int unit0 = (split >> 1) * 64 + ((split & 1) ? kUnitsEven : 0);
  const int nunit = (nunit_wg - wave + 7) >> 3;
  const size_t kv_off = ((size_t)(b * kHkv + kvh) * kSk + (size_t)(unit0 + wave) * 64) * kDh;
  const unsigned short* kbase = p.k[br] + kv_off;
  const unsigned short* vbase = p.v[br] + kv_off;
  const int kv_bytes = (nunit - 1) * 131072 + 16384;
  const rsrc_t rq = make_rsrc(p.wq[br] + kvh * 1024 + qblk * 32, (kD - 1) * kHq * kDh * 2 + 64);
  const rsrc_t rx = make_rsrc(p.x[br], 2 * kD * 2);
  const rsrc_t ro = make_rsrc(p.wo[br] + (size_t)((kvh * 8 + hl) * kDh + rh * 64) * kD + cb * 512, 63 * kD * 2 + 1024);

  const v2u ep = __builtin_amdgcn_raw_buffer_load_b64(rws, 0, WS_EPOCH + br * 256, AUX_SC1);
  const int op_slot = WS_OP + ((br * 2 + cb) * 64 + (kvh * 16 + (idx >> 1))) * 4096;
  {
    const v4u s4 = {kSent, kSent, kSent, kSent};
    const int off = tid < 256 ? WS_PART + (pair * 32 + idx) * 4096 + tid * 16 : op_slot + (tid - 256) * 16;
    __builtin_amdgcn_raw_buffer_store_b128(s4, rws, off, 0, AUX_SC1);
    if (lane == 0) {
      const v2u s2 = {kSent, kSent};
      __builtin_amdgcn_raw_buffer_store_b64(s2, rws, WS_ML + ((pair * 8 + wave) * 32 + idx) * 8, 0, AUX_SC1);
    }
  }
  PIN();
  v4u xw[2], wqr[8];
  {
    const int xo = (128 * wave + 8 * (lane >> 2)) * 2;
    xw[0] = ld16<0>(rx, xo, 0);
    xw[1] = ld16<0>(rx, xo, kD * 2);
    const int qo = (lane >> 2) * 8 * 8192 + (lane & 3) * 16;
#pragma unroll
    for (int i = 0; i < 8; ++i) wqr[i] = ld16<AUX_NT>(rq, qo, (128 * wave + i) * 8192);
  }
  PIN();
  const int kl = (lane & 15) >> 1;
  int koff = (4 * (kl & 1) + (kl >> 1)) * 256 + (4 * (lane & 1) + (lane >> 4)) * 16;
  int voff = lane * 16;
  v4u Kt[2][8], Vt[2][8];
#define LOAD_K(buf, u)                                                                                               \
  do {                                                                                                               \
    const rsrc_t r_ = make_rsrc(kbase, (u) < nunit ? kv_bytes : 0);                                                  \
    asm volatile("" : "+v"(koff));                                                                                   \
    _Pragma("unroll") for (int i = 0; i < 8; ++i)                                                                    \
        Kt[buf][i] = ld16<AUX_NT>(r_, koff + (i & 2) * 1024 + (i & 1) * 128, (u) * 131072 + (buf) * 8192 + (i >> 2) * 4096); \
    PIN();                                                                                                           \
  } while (0)
#define LOAD_V(buf, u)                                                                                               \
  do {                                                                                                               \
    const rsrc_t r_ = make_rsrc(vbase, (u) < nunit ? kv_bytes : 0);                                                  \
    asm volatile("" : "+v"(voff));                                                                                   \
    _Pragma("unroll") for (int i = 0; i < 8; ++i)                                                                    \
        Vt[buf][i] = ld16<AUX_NT>(r_, voff + (i & 3) * 1024, (u) * 131072 + (buf) * 8192 + (i >> 2) * 4096);          \
    PIN();                                                                                                           \
  } while (0)
  __builtin_amdgcn_s_barrier();
  PIN();
  LOAD_K(0, 0);

  {
    float qa[16];
#pragma unroll
    for (int e = 0; e < 8; ++e) {
      const v4u c = column_of(wqr, e);
#pragma unroll
      for (int bb = 0; bb < 2; ++bb) {
        float a = 0.f;
#pragma unroll
        for (int u = 0; u < 4; ++u) a = dot2(c[u], xw[bb][u], a);
        qa[bb * 8 + e] = a;
      }
    }
    float r8[8], r4[4];
#pragma unroll
    for (int e = 0; e < 8; ++e) r8[e] = swap32_add(qa[e], qa[8 + e]);
#pragma unroll
    for (int e = 0; e < 4; ++e) r4[e] = swap16_add(r8[e], r8[4 + e]);
    const bool up = lane & 8;
    const float o0 = (up ? r4[2] : r4[0]) + dpp<ROR8>(up ? r4[0] : r4[2]);
    const float o1 = (up ? r4[3] : r4[1]) + dpp<ROR8>(up ? r4[1] : r4[3]);
    asm volatile("s_waitcnt vmcnt(8)" ::: "memory");
    STAMP(1);
    *reinterpret_cast<float2*>(lds + L_QRED + wave * 512 + lane * 8) = make_float2(o0, o1);
  }
  wg_barrier();
  const u64 epoch = ((u64)(unsigned)__builtin_amdgcn_readfirstlane(ep[1]) << 32) | (unsigned)__builtin_amdgcn_readfirstlane(ep[0]);
  const u64 tag = mix64(epoch) & ~0xFFFFull;
  const unsigned tag_lo = (unsigned)tag, tag_hi = (unsigned)(tag >> 32);
  if (wave == 0) {
    __builtin_amdgcn_s_setprio(3);
    const int bb = lane >> 5, col = lane & 31;
    const int src = bb * 32 + ((col >> 2) & 1) * 16 + ((col >> 1) & 1) * 8 + (col >> 3);
    float s = 0.f;
#pragma unroll
    for (int w = 0; w < 8; ++w) {
      const float* q = reinterpret_cast<const float*>(lds + L_QRED + w * 512);
      s += q[src * 2 + (col & 1)] + q[(src + 4) * 2 + (col & 1)];
    }
    const v2u g = {tag_lo | (pack_bf16(s, 0.f) & 0xFFFFu), tag_hi};
    __builtin_amdgcn_raw_buffer_store_b64(g, rws, WS_QG + ((pair * 2 + bb) * 1024 + qblk * 32 + col) * 8, 0, AUX_SC1);
    STAMP(2);
    const int qg_off = WS_QG + (pair * 2 + b) * 8192 + lane * 16;
    v4u qg[8];
    unsigned spins = 0;
    unsigned bad;
    do {
      MEMFENCE();
#pragma unroll
      for (int i = 0; i < 8; ++i) qg[i] = ld16<AUX_SC1>(rws, qg_off, i * 1024);
      unsigned bl = 0u, bh = 0u;
#pragma unroll
      for (int i = 0; i < 8; ++i) {
        bl |= (qg[i][0] ^ tag_lo) | (qg[i][2] ^ tag_lo);
        bh |= (qg[i][1] ^ tag_hi) | (qg[i][3] ^ tag_hi);
      }
      bad = (bl >> 16) | bh;
    } while (__any(bad != 0u) && ++spins < kSpinCap);
#pragma unroll
    for (int i = 0; i < 8; ++i) *reinterpret_cast<unsigned*>(lds + L_QIMG + i * 256 + lane * 4) = perm_lo(qg[i][2], qg[i][0]);
    STAMP(3);
    wg_barrier();
    __builtin_amdgcn_s_setprio(0);
  }
  PIN();
  LOAD_V(0, 0);
  LOAD_K(1, 0);
  LOAD_V(1, 0);
  if (wave != 0) {
    STAMP(3);
    wg_barrier();
  }
  STAMP(4);
  const int pp = (lane >> 3) & 1, hh = lane & 7, gg = lane >> 4;
  const v4u Q0 = *reinterpret_cast<const v4u*>(lds + L_QIMG + hh * 256 + pp * 64 + gg * 16);
  const v4u Q1 = *reinterpret_cast<const v4u*>(lds + L_QIMG + hh * 256 + 128 + pp * 64 + gg * 16);

  STAMP(5);
  f32x4 acc[8];
#pragma unroll
  for (int t = 0; t < 8; ++t) acc[t] = f32x4{0.f, 0.f, 0.f, 0.f};
  float m = -INFINITY, l = 0.f;
  const unsigned pmask = pp ? 0xFFFF0000u : 0x0000FFFFu;
#define TILE(buf, u)                                                                                                 \
  do {                                                                                                               \
    float s[4];                                                                                                      \
    _Pragma("unroll") for (int T = 0; T < 4; ++T) {                                                                  \
      f32x4 d = mfma16(Kt[buf][2 * T], Q0, f32x4{0.f, 0.f, 0.f, 0.f});                                               \
      d = mfma16(Kt[buf][2 * T + 1], Q1, d);                                                                         \
      s[T] = (pp ? d[3] : d[0]) + dpp<ROR8>(pp ? d[1] : d[2]);                                                       \
    }                                                                                                                \
    PIN();                                                                                                           \
    LOAD_K(buf, (u) + 1);                                                                                            \
    float mt = fmaxf(fmaxf(s[0], s[1]), fmaxf(s[2], s[3]));                                                          \
    mt = fmaxf(mt, dpp<ROR8>(mt));                                                                                   \
    mt = swap32_max(swap16_max(mt)) * kC2;                                                                           \
    const float mn = fmaxf(m, mt);                                                                                   \
    if (__any(mn != m)) {                                                                                            \
      const float alpha = __builtin_amdgcn_exp2f(m - mn);                                                            \
      l *= alpha;                                                                                                    \
      _Pragma("unroll") for (int i = 0; i < 8; ++i) acc[i] *= alpha;                                                 \
      m = mn;                                                                                                        \
    }                                                                                                                \
    v4u P;                                                                                                           \
    _Pragma("unroll") for (int T = 0; T < 4; ++T) {                                                                  \
      const float e = __builtin_amdgcn_exp2f(fmaf(s[T], kC2, -m));                                                   \
      l += e;                                                                                                        \
      P[T] = pack_bf16(e, e) & pmask;                                                                                \
    }                                                                                                                \
    _Pragma("unroll") for (int i = 0; i < 8; ++i) acc[i] = mfma16(column_of(Vt[buf], i), P, acc[i]);                 \
    PIN();                                                                                                           \
    LOAD_V(buf, (u) + 1);                                                                                            \
  } while (0)
  for (int u = 0; u < nunit; ++u) {
    TILE(0, u);
    if (u == 0) STAMP(6);
    TILE(1, u);
  }
#undef TILE
  STAMP(12);
  v4u wor[8];
#pragma unroll
  for (int i = 0; i < 8; ++i) wor[i] = ld16<AUX_NT>(ro, lane * 16, (8 * wave + i) * 2048);
  PIN();
  STAMP(13);
#undef LOAD_K
#undef LOAD_V

  l += dpp<ROR8>(l);
  l = swap16_add(l, l);
  l = swap32_add(l, l);
  {
    float* dst = reinterpret_cast<float*>(lds + L_MRG) + (wave * 8 + hh) * 132 + 32 * gg + 16 * pp;
#pragma unroll
    for (int rr = 0; rr < 2; ++rr)
#pragma unroll
      for (int q = 0; q < 2; ++q) {
        f32x4 o;
#pragma unroll
        for (int e = 0; e < 4; ++e) {
          const f32x4 a = acc[4 * q + e];
          o[e] = (pp ? a[2 + rr] : a[rr]) + dpp<ROR8>(pp ? a[rr] : a[2 + rr]);
        }
        *reinterpret_cast<f32x4*>(dst + 8 * rr + 4 * q) = o;
      }
    if (lane < 8) {
      float* ml = reinterpret_cast<float*>(lds + L_MRGML) + lane * 16;
      ml[wave] = m;
      ml[8 + wave] = l;
    }
  }
  wg_barrier();
  {
    const float* ml = reinterpret_cast<const float*>(lds + L_MRGML) + wave * 16;
    const f32x4 m0 = *reinterpret_cast<const f32x4*>(ml), m1 = *reinterpret_cast<const f32x4*>(ml + 4);
    const f32x4 l0 = *reinterpret_cast<const f32x4*>(ml + 8), l1 = *reinterpret_cast<const f32x4*>(ml + 12);
    const float M = fmaxf(fmaxf(fmaxf(m0[0], m0[1]), fmaxf(m0[2], m0[3])), fmaxf(fmaxf(m1[0], m1[1]), fmaxf(m1[2], m1[3])));
    float L = 0.f, o0 = 0.f, o1 = 0.f;
#pragma unroll
    for (int w = 0; w < 8; ++w) {
      const float e = __builtin_amdgcn_exp2f((w < 4 ? m0[w & 3] : m1[w & 3]) - M);
      const float2 o = *reinterpret_cast<const float2*>(lds + L_MRG + ((w * 8 + wave) * 132 + 2 * lane) * 4);
      L = fmaf(e, w < 4 ? l0[w & 3] : l1[w & 3], L);
      o0 = fmaf(e, o.x, o0);
      o1 = fmaf(e, o.y, o1);
    }
    const v2u ov = {not_sentinel(o0), not_sentinel(o1)};
    __builtin_amdgcn_raw_buffer_store_b64(ov, rws, WS_PART + (pair * 32 + idx) * 4096 + wave * 512 + lane * 8, 0, AUX_SC1);
    if (lane == 0) {
      const v2u mv = {not_sentinel(M), not_sentinel(L)};
      __builtin_amdgcn_raw_buffer_store_b64(mv, rws, WS_ML + ((pair * 8 + wave) * 32 + idx) * 8, 0, AUX_SC1);
    }
  }
  PIN();
  STAMP(14);

  v4u wc[8];
#pragma unroll
  for (int e = 0; e < 8; ++e) wc[e] = column_of(wor, e);
  PIN();
  v2u chk = {tag_lo, tag_hi};
  const int chk_idx = 2 * (lane & 15) + cb;
  const int chk_blk = ((chk_idx & 7) * 2 + ((chk_idx >> 3) & 1)) + (chk_idx & 16);
  const int chk_off = WS_QG + ((br * 4 + (lane >> 4)) * 2 * 1024 + chk_blk * 32) * 8;
  if (wave == 0) chk = __builtin_amdgcn_raw_buffer_load_b64(rws, chk_off, 0, AUX_SC1);
  const int po = WS_PART + (pair * 32 + (lane >> 1)) * 4096 + (hl * 128 + rh * 64 + 8 * wave + 4 * (lane & 1)) * 4;
  const int mo = WS_ML + ((pair * 8 + hl) * 32 + (lane >> 1)) * 8;
  v4u pd = ld16<AUX_SC1>(rws, po, 0);
  v2u pm = __builtin_amdgcn_raw_buffer_load_b64(rws, mo, 0, AUX_SC1);
  for (unsigned spins = 0;; ++spins) {
    const bool ok = valid4(pd) && pm[0] != kSent && pm[1] != kSent;
    if (__all(ok) || spins > kSpinCap) break;
    __builtin_amdgcn_s_sleep(1);
    MEMFENCE();
    if (!ok) {
      pd = ld16<AUX_SC1>(rws, po, 0);
      pm = __builtin_amdgcn_raw_buffer_load_b64(rws, mo, 0, AUX_SC1);
    }
  }
  STAMP(15);
  {
    const float ms = __uint_as_float(pm[0]), ls = __uint_as_float(pm[1]);
    float M = fmaxf(ms, dpp<ROR2>(ms));
    M = fmaxf(M, dpp<ROR4>(M));
    M = fmaxf(M, dpp<ROR8>(M));
    M = swap16_max(M);
    const float e = __builtin_amdgcn_exp2f(ms - M);
    float v[5] = {e * ls, e * __uint_as_float(pd[0]), e * __uint_as_float(pd[1]), e * __uint_as_float(pd[2]), e * __uint_as_float(pd[3])};
#pragma unroll
    for (int i = 0; i < 5; ++i) {
      float y = v[i];
      y += dpp<ROR2>(y);
      y += dpp<ROR4>(y);
      y += dpp<ROR8>(y);
      v[i] = swap16_add(y, y);
    }
    const float inv = 1.f / v[0];
    const unsigned o01 = pack_bf16(v[1] * inv, v[2] * inv), o23 = pack_bf16(v[3] * inv, v[4] * inv);
    float oa[2][8];
#pragma unroll
    for (int bb = 0; bb < 2; ++bb) {
      const unsigned s0 = __builtin_amdgcn_readlane(o01, 32 * bb), s1 = __builtin_amdgcn_readlane(o23, 32 * bb);
      const unsigned s2 = __builtin_amdgcn_readlane(o01, 32 * bb + 1), s3 = __builtin_amdgcn_readlane(o23, 32 * bb + 1);
#pragma unroll
      for (int e2 = 0; e2 < 8; ++e2)
        oa[bb][e2] = dot2(wc[e2][3], s3, dot2(wc[e2][2], s2, dot2(wc[e2][1], s1, dot2(wc[e2][0], s0, 0.f))));
    }
    float* dst = reinterpret_cast<float*>(lds + L_ORED) + wave * 1024 + lane * 4;
#pragma unroll
    for (int bb = 0; bb < 2; ++bb)
#pragma unroll
      for (int q = 0; q < 2; ++q)
        *reinterpret_cast<f32x4*>(dst + bb * 512 + q * 256) = f32x4{oa[bb][4 * q], oa[bb][4 * q + 1], oa[bb][4 * q + 2], oa[bb][4 * q + 3]};
  }
  wg_barrier();
  {
    float o0 = 0.f, o1 = 0.f;
#pragma unroll
    for (int w = 0; w < 8; ++w) {
      const float2 o = *reinterpret_cast<const float2*>(lds + L_ORED + w * 4096 + tid * 8);
      o0 += o.x;
      o1 += o.y;
    }
    const int bb = tid >> 8, q = (tid >> 7) & 1, ln = (tid >> 1) & 63, e = (tid & 1) * 2;
    const v2u ov = {not_sentinel(o0), not_sentinel(o1)};
    __builtin_amdgcn_raw_buffer_store_b64(ov, rws, op_slot + (bb * 512 + 8 * ln + 4 * q + e) * 4, 0, AUX_SC1);
  }
  STAMP(16);
  if (wave != 0) { STAMP_FLUSH(); return; }

  for (unsigned spins = 0;; ++spins) {
    const bool ok = ((chk[0] ^ tag_lo) >> 16) == 0u && chk[1] == tag_hi;
    if (__all(ok) || spins > kSpinCap) break;
    __builtin_amdgcn_s_sleep(2);
    MEMFENCE();
    chk = __builtin_amdgcn_raw_buffer_load_b64(rws, chk_off, 0, AUX_SC1);
  }
  MEMFENCE();
  {
    const int r = kvh * 16 + (idx >> 1);
    const int ob = r >> 5, cg = r & 31;
    const int fo = WS_OP + ((br * 2 + cb) * 64 + (lane >> 2)) * 4096 + (ob * 512 + cg * 16 + (lane & 3) * 4) * 4;
    v4u f[4];
#pragma unroll
    for (int q = 0; q < 4; ++q) f[q] = ld16<AUX_SC1>(rws, fo, q * 16 * 4096);
    for (unsigned spins = 0;; ++spins) {
      const bool ok = valid4(f[0]) && valid4(f[1]) && valid4(f[2]) && valid4(f[3]);
      if (__all(ok) || spins > kSpinCap) break;
      __builtin_amdgcn_s_sleep(1);
      MEMFENCE();
      if (!ok) {
#pragma unroll
        for (int q = 0; q < 4; ++q) f[q] = ld16<AUX_SC1>(rws, fo, q * 16 * 4096);
      }
    }
    STAMP(17);
    float y[4];
#pragma unroll
    for (int e = 0; e < 4; ++e) {
      float s = (__uint_as_float(f[0][e]) + __uint_as_float(f[1][e])) + (__uint_as_float(f[2][e]) + __uint_as_float(f[3][e]));
      s += dpp<ROR4>(s);
      s += dpp<ROR8>(s);
      s = swap16_add(s, s);
      y[e] = swap32_add(s, s);
    }
    if (lane < 4) {
      const v2u o = {pack_bf16(y[0], y[1]), pack_bf16(y[2], y[3])};
      *reinterpret_cast<v2u*>(p.out + ob * 2048 + br * 1024 + cb * 512 + cg * 16 + lane * 4) = o;
    }
    if (kvh == 0 && idx == 0 && lane == 0) {
      const u64 nx = epoch + 1;
      const v2u e2 = {(unsigned)nx, (unsigned)(nx >> 32)};
      __builtin_amdgcn_raw_buffer_store_b64(e2, rws, WS_EPOCH + br * 256, 0, AUX_SC1);
    }
    STAMP(18);
    STAMP_FLUSH();
  }
}

}

extern "C" void kernel_launch(void* const* d_in, const int* in_sizes, int n_in, void* d_out, int out_size, void* d_ws,
                              size_t ws_size, hipStream_t stream) {
  (void)in_sizes; (void)n_in; (void)out_size; (void)ws_size;
  Params p;
  for (int i = 0; i < 2; ++i) {
    p.x[i] = (const unsigned short*)d_in[5 * i + 0];
    p.k[i] = (const unsigned short*)d_in[5 * i + 1];
    p.v[i] = (const unsigned short*)d_in[5 * i + 2];
    p.wq[i] = (const unsigned short*)d_in[5 * i + 3];
    p.wo[i] = (const unsigned short*)d_in[5 * i + 4];
  }
  p.out = (unsigned short*)d_out;
  p.ws = (unsigned char*)d_ws;
  hipLaunchKernelGGL(fused_decode, dim3(kNWG), dim3(kNThr), 0, stream, p);
}
```

```cpp
#include <hip/hip_runtime.h>
#include <hip/hip_bf16.h>
#include <stdint.h>

typedef unsigned v4u __attribute__((ext_vector_type(4)));
typedef unsigned v2u __attribute__((ext_vector_type(2)));
typedef float f32x4 __attribute__((ext_vector_type(4)));
typedef __bf16 bf16x8 __attribute__((ext_vector_type(8)));
typedef __bf16 bf16x2 __attribute__((ext_vector_type(2)));
typedef unsigned long long u64;
typedef __amdgpu_buffer_rsrc_t rsrc_t;

namespace {

constexpr int kD = 1024, kHkv = 4, kSk = 32768, kDh = 128;
constexpr int kNWG = 256, kNThr = 512;
constexpr int kUnitsS = 7;
#define SCHED_T0 400
#define SCHED_TAU 460
constexpr int AUX_NT = 2, AUX_SC1 = 16;
constexpr int RSRC_W3 = 0x00020000;
constexpr unsigned kSent = 0xFFFFFFFFu;
constexpr int kDrop = 0x7FFFFF00;
constexpr unsigned kSpinCap = 1u << 18;
constexpr float kC2 = 0.088388347648318440f * 1.4426950408889634f;

constexpr int WS_EPOCH = 0;
constexpr int WS_RDY = 1024;
constexpr int WS_QG = 4096;
constexpr int kPartSlot = 4096;
constexpr int WS_PART = WS_QG + 8 * 2 * 2 * 1024 * 8;
constexpr int WS_OP = WS_PART + 256 * kPartSlot;
constexpr int WS_END = WS_OP + 256 * 8192;

constexpr int L_X = 0;
constexpr int L_TAG = L_X + 2048;
constexpr int L_CNT = L_TAG + 16;
constexpr int L_QRED = L_CNT + 32;
constexpr int L_QIMG = L_QRED + 4 * 512;
constexpr int L_OIMG = L_QIMG + 2048;
constexpr int L_MRGML = L_OIMG + 128;
constexpr int L_MRG = L_MRGML + 8 * 16 * 4;
constexpr int L_END = L_MRG + 8 * 8 * 132 * 4;

enum { C_QPART, C_QIMG, C_RESET, C_LAST, C_PART, C_TICKET, C_T0 };

struct Params {
  const unsigned short* x[2];
  const unsigned short* k[2];
  const unsigned short* v[2];
  const unsigned short* wq[2];
  const unsigned short* wo[2];
  unsigned short* out;
  unsigned char* ws;
};

__device__ __forceinline__ rsrc_t make_rsrc(const void* p, int bytes) {
  return __builtin_amdgcn_make_buffer_rsrc((void*)p, 0, bytes, RSRC_W3);
}
template <int AUX> __device__ __forceinline__ v4u ld16(rsrc_t r, int voff, int soff) {
  return __builtin_amdgcn_raw_buffer_load_b128(r, voff, soff, AUX);
}
__device__ __forceinline__ f32x4 mfma16(v4u a, v4u b, f32x4 c) {
  return __builtin_amdgcn_mfma_f32_16x16x32_bf16(__builtin_bit_cast(bf16x8, a), __builtin_bit_cast(bf16x8, b), c, 0, 0, 0);
}
__device__ __forceinline__ float dot2(unsigned a, unsigned b, float c) {
  return __builtin_amdgcn_fdot2_f32_bf16(__builtin_bit_cast(bf16x2, a), __builtin_bit_cast(bf16x2, b), c, false);
}
__device__ __forceinline__ unsigned pack_bf16(float lo, float hi) {
  bf16x2 r = {(__bf16)lo, (__bf16)hi};
  return __builtin_bit_cast(unsigned, r);
}
template <int CTRL> __device__ __forceinline__ float dpp(float x) {
  return __builtin_bit_cast(float, __builtin_amdgcn_mov_dpp(__builtin_bit_cast(int, x), CTRL, 0xf, 0xf, true));
}
constexpr int ROR2 = 0x122, ROR4 = 0x124, ROR8 = 0x128;
__device__ __forceinline__ float swap16_add(float a, float b) {
  auto s = __builtin_amdgcn_permlane16_swap(__float_as_uint(a), __float_as_uint(b), false, false);
  return __uint_as_float(s[0]) + __uint_as_float(s[1]);
}
__device__ __forceinline__ float swap32_add(float a, float b) {
  auto s = __builtin_amdgcn_permlane32_swap(__float_as_uint(a), __float_as_uint(b), false, false);
  return __uint_as_float(s[0]) + __uint_as_float(s[1]);
}
__device__ __forceinline__ float swap16_max(float a) {
  auto s = __builtin_amdgcn_permlane16_swap(__float_as_uint(a), __float_as_uint(a), false, false);
  return fmaxf(__uint_as_float(s[0]), __uint_as_float(s[1]));
}
__device__ __forceinline__ float swap32_max(float a) {
  auto s = __builtin_amdgcn_permlane32_swap(__float_as_uint(a), __float_as_uint(a), false, false);
  return fmaxf(__uint_as_float(s[0]), __uint_as_float(s[1]));
}
__device__ __forceinline__ unsigned perm_lo(unsigned hi, unsigned lo) { return __builtin_amdgcn_perm(hi, lo, 0x05040100u); }
__device__ __forceinline__ unsigned perm_hi(unsigned hi, unsigned lo) { return __builtin_amdgcn_perm(hi, lo, 0x07060302u); }
__device__ __forceinline__ v4u column_of(const v4u* r, int t) {
  v4u o;
#pragma unroll
  for (int u = 0; u < 4; ++u)
    o[u] = (t & 1) ? perm_hi(r[2 * u + 1][t >> 1], r[2 * u][t >> 1]) : perm_lo(r[2 * u + 1][t >> 1], r[2 * u][t >> 1]);
  return o;
}
__device__ __forceinline__ unsigned not_sentinel(float f) {
  const unsigned u = __float_as_uint(f);
  return u == kSent ? 0x7FC00000u : u;
}
__device__ __forceinline__ unsigned quiet(float f) { return f != f ? 0x7FC00000u : __float_as_uint(f); }
__device__ __forceinline__ unsigned umax4(v4u a) { return max(max(a[0], a[1]), max(a[2], a[3])); }
__device__ __forceinline__ u64 mix64(u64 z) {
  z += 0x9E3779B97F4A7C15ull;
  z = (z ^ (z >> 30)) * 0xBF58476D1CE4E5B9ull;
  z = (z ^ (z >> 27)) * 0x94D049BB133111EBull;
  return z ^ (z >> 31);
}
#define STAMP(i) do {} while (0)
#define STAMP_FLUSH() do {} while (0)
#define PIN() __builtin_amdgcn_sched_barrier(0)
#define MEMFENCE() asm volatile("" ::: "memory")
__device__ __forceinline__ void wg_barrier() {
  asm volatile("s_waitcnt lgkmcnt(0)" ::: "memory");
  __builtin_amdgcn_s_barrier();
  asm volatile("" ::: "memory");
}

__device__ __forceinline__ void lds_arrive(unsigned* c, int lane) {
  asm volatile("s_waitcnt lgkmcnt(0)" ::: "memory");
  if (lane == 0) __hip_atomic_fetch_add(c, 1u, __ATOMIC_RELAXED, __HIP_MEMORY_SCOPE_WORKGROUP);
}
template <int SLEEP = 0> __device__ __forceinline__ void lds_wait(unsigned* c, unsigned n) {
  while (__hip_atomic_load(c, __ATOMIC_RELAXED, __HIP_MEMORY_SCOPE_WORKGROUP) < n) __builtin_amdgcn_s_sleep(SLEEP);
  MEMFENCE();
}

__global__ void __launch_bounds__(kNThr, 2) fused_decode(Params p) {
  __shared__ __attribute__((aligned(16))) unsigned char lds[L_END];
  const int tid = threadIdx.x, lane = tid & 63;
  const int wave = __builtin_amdgcn_readfirstlane(tid >> 6);
  const bool streamer = wave < 4;
  const int wg = blockIdx.x, pair = wg >> 5, idx = wg & 31;
  const int br = pair >> 2, kvh = pair & 3, b = idx >> 4, split = idx & 15;
  const int qblk = idx & 15, kh = idx >> 4;
  const int hl = idx >> 2, rq = idx & 3;
  STAMP(0);
  const unsigned t_in = (unsigned)__builtin_amdgcn_s_memrealtime();
  PIN();
  const rsrc_t rws = make_rsrc(p.ws, WS_END);
  const size_t kv_off = ((size_t)(b * kHkv + kvh) * kSk + (size_t)(split * 64 + (streamer ? wave : 8 * kUnitsS - 4 + wave)) * 32) * kDh;
  const rsrc_t rk = make_rsrc(p.k[br] + kv_off, streamer ? (2 * kUnitsS - 1) * 32768 + 8192 : 32768 + 8192);
  const rsrc_t rv = make_rsrc(p.v[br] + kv_off, streamer ? (2 * kUnitsS - 1) * 32768 + 8192 : 32768 + 8192);

  unsigned* const cnt = reinterpret_cast<unsigned*>(lds + L_CNT);
  v4u wqr[16], xv;
  v2u ep;
  if (!streamer) {
    const int xw = wave - 4;
    const rsrc_t rq_ = make_rsrc(p.wq[br] + (size_t)(kh * 512 + xw * 128) * 4096 + kvh * 1024 + qblk * 64, 127 * 8192 + 128);
    const rsrc_t rx = make_rsrc(p.x[br] + kh * 512 + xw * 128, 2048 + 256);
    const int qo = (lane >> 3) * 16 * 8192 + (lane & 7) * 16;
    ep = __builtin_amdgcn_raw_buffer_load_b64(rws, wave == 7 ? WS_EPOCH + br * 256 : kDrop, 0, AUX_SC1);
    xv = ld16<0>(rx, lane < 32 ? (lane >> 4) * 2048 + (lane & 15) * 16 : kDrop, 0);
#pragma unroll
    for (int i = 0; i < 16; ++i) wqr[i] = ld16<AUX_NT>(rq_, qo, i * 8192);
    if (tid < 256 + 8) cnt[lane] = lane == C_T0 ? t_in : 0u;
  }
  PIN();
  wg_barrier();
  PIN();
  const int kl = (lane & 15) >> 1;
  int koff = (4 * (kl & 1) + (kl >> 1)) * 256 + (4 * (lane & 1) + (lane >> 4)) * 16;
  int voff = lane * 16;
  v4u Kt[2][8], Vt[2][8];
#define K_T0 SCHED_T0
#define K_TAU SCHED_TAU
#define K_GATE_WO 1
#define K_LAST 4u
#define GROUP_CAP() asm volatile("s_waitcnt vmcnt(4)" ::: "memory")
#define K_LIGHT 0
#define K_EARLY_RESET 1
#define K_TOUCH 1
#define NAP(n) do { for (int z_ = 0; z_ < (n); ++z_) __builtin_amdgcn_s_sleep(2); } while (0)
#define K_D1 12
#define K_D2 20
#define K_DP 4
  const unsigned t_gate = __builtin_amdgcn_readfirstlane(cnt[C_T0]) + K_T0;
#define GATE_(CAP)                                                                                                   \
  do {                                                                                                               \
    unsigned tk_ = 0;                                                                                                \
    if (lane == 0) tk_ = __hip_atomic_fetch_add(cnt + C_TICKET, 1u, __ATOMIC_RELAXED, __HIP_MEMORY_SCOPE_WORKGROUP); \
    const unsigned due_ = t_gate + ((__builtin_amdgcn_readfirstlane(tk_) * K_TAU) >> 4);                             \
    CAP;                                                                                                             \
    while ((int)((unsigned)__builtin_amdgcn_s_memrealtime() - due_) < 0) __builtin_amdgcn_s_sleep(1);               \
    MEMFENCE();                                                                                                      \
  } while (0)
#define GATE() GATE_(GROUP_CAP())
#define LOAD_K(buf, u)                                                                                               \
  do {                                                                                                               \
    GATE();                                                                                                          \
    asm volatile("" : "+v"(koff));                                                                                   \
    _Pragma("unroll") for (int i = 0; i < 8; ++i)                                                                    \
        Kt[buf][i] = ld16<AUX_NT>(rk, koff + (i & 2) * 1024 + (i & 1) * 128, (u) * 65536 + (buf) * 32768 + (i >> 2) * 4096); \
    PIN();                                                                                                           \
  } while (0)
#define LOAD_V(buf, u)                                                                                               \
  do {                                                                                                               \
    GATE();                                                                                                          \
    asm volatile("" : "+v"(voff));                                                                                   \
    _Pragma("unroll") for (int i = 0; i < 8; ++i)                                                                    \
        Vt[buf][i] = ld16<AUX_NT>(rv, voff + (i & 3) * 1024, (u) * 65536 + (buf) * 32768 + (i >> 2) * 4096);         \
    PIN();                                                                                                           \
  } while (0)
  if (streamer) {
    if (K_TOUCH) {
      const rsrc_t ro_ = make_rsrc(p.wo[br] + (size_t)((kvh * 8 + hl) * kDh + rq * 32) * kD, 32 * kD * 2);
      const int to = lane == 0 ? 0 : kDrop;
      unsigned t0_ = __builtin_amdgcn_raw_buffer_load_b32(rk, to, wave * 98304, AUX_NT);
      unsigned t1_ = __builtin_amdgcn_raw_buffer_load_b32(rv, to, wave * 98304, AUX_NT);
      unsigned t2_ = __builtin_amdgcn_raw_buffer_load_b32(ro_, to, wave * 16384, AUX_NT);
      asm volatile("" ::"v"(t0_), "v"(t1_), "v"(t2_));
    }
    if (K_EARLY_RESET) {
      const v4u s4 = {kSent, kSent, kSent, kSent};
      __builtin_amdgcn_raw_buffer_store_b128(s4, rws, WS_OP + wg * 8192 + tid * 16, 0, AUX_SC1);
      __builtin_amdgcn_raw_buffer_store_b128(s4, rws, WS_OP + wg * 8192 + 4096 + tid * 16, 0, AUX_SC1);
      __builtin_amdgcn_raw_buffer_store_b128(s4, rws, WS_PART + wg * kPartSlot + tid * 16, 0, AUX_SC1);
      asm volatile("s_waitcnt vmcnt(0)" ::: "memory");
      lds_arrive(cnt + C_RESET, lane);
    }
  } else {
    const int xw = wave - 4;
    if (lane < 32) *reinterpret_cast<v4u*>(lds + L_X + xw * 512 + lane * 16) = xv;
    asm volatile("s_waitcnt lgkmcnt(0)" ::: "memory");
    float qa[16];
#pragma unroll
    for (int i = 0; i < 16; ++i) qa[i] = 0.f;
#pragma unroll
    for (int h = 0; h < 2; ++h) {
      const v4u x0 = *reinterpret_cast<const v4u*>(lds + L_X + xw * 512 + (lane >> 3) * 32 + h * 16);
      const v4u x1 = *reinterpret_cast<const v4u*>(lds + L_X + xw * 512 + 256 + (lane >> 3) * 32 + h * 16);
#pragma unroll
      for (int e = 0; e < 8; ++e) {
        const v4u c = column_of(wqr + 8 * h, e);
#pragma unroll
        for (int u = 0; u < 4; ++u) {
          qa[e] = dot2(c[u], x0[u], qa[e]);
          qa[8 + e] = dot2(c[u], x1[u], qa[8 + e]);
        }
      }
    }
    float r8[8], r4[4];
#pragma unroll
    for (int e = 0; e < 8; ++e) r8[e] = swap32_add(qa[e], qa[8 + e]);
#pragma unroll
    for (int e = 0; e < 4; ++e) r4[e] = swap16_add(r8[e], r8[4 + e]);
    const bool up = lane & 8;
    const float o0 = (up ? r4[2] : r4[0]) + dpp<ROR8>(up ? r4[0] : r4[2]);
    const float o1 = (up ? r4[3] : r4[1]) + dpp<ROR8>(up ? r4[1] : r4[3]);
    *reinterpret_cast<float2*>(lds + L_QRED + xw * 512 + lane * 8) = make_float2(o0, o1);
    STAMP(1);
    lds_arrive(cnt + C_QPART, lane);
  }
  if (wave == 7) {
    const u64 epoch = ((u64)(unsigned)__builtin_amdgcn_readfirstlane(ep[1]) << 32) | (unsigned)__builtin_amdgcn_readfirstlane(ep[0]);
    const u64 tg = mix64(epoch);
    const unsigned t8 = (unsigned)tg & 0xFF000000u, th = (unsigned)(tg >> 32);
    if (lane == 0) *reinterpret_cast<v4u*>(lds + L_TAG) = v4u{(unsigned)epoch, (unsigned)(epoch >> 32), (unsigned)tg, th};
    lds_wait(cnt + C_QPART, 4u);
    float s0 = 0.f, s1 = 0.f;
#pragma unroll
    for (int w = 0; w < 4; ++w) {
      const float2 q = *reinterpret_cast<const float2*>(lds + L_QRED + w * 512 + lane * 8);
      s0 += q.x;
      s1 += q.y;
    }
    const v4u g = {t8 | (__float_as_uint(s0) >> 8), th, t8 | (__float_as_uint(s1) >> 8), th};
    const int col = 8 * (lane & 7) + 4 * ((lane >> 4) & 1) + 2 * ((lane >> 3) & 1);
    __builtin_amdgcn_raw_buffer_store_b128(g, rws, WS_QG + (((pair * 2 + (lane >> 5)) * 2 + kh) * 1024 + qblk * 64 + col) * 8, 0, AUX_SC1);
    STAMP(2);
    const int qg_off = WS_QG + (pair * 2 + b) * 16384 + lane * 16;
    v4u qg[16];
    unsigned spins = 0;
    unsigned bad;
    if (K_LIGHT) {
      const int lo = lane < 32 ? WS_QG + (pair * 2 + b) * 16384 + lane * 512 : kDrop;
      do {
        MEMFENCE();
        const v2u g1 = __builtin_amdgcn_raw_buffer_load_b64(rws, lo, 0, AUX_SC1);
        bad = lane < 32 ? ((g1[0] ^ t8) >> 24) | (g1[1] ^ th) : 0u;
      } while (__any(bad != 0u) && ++spins < kSpinCap);
      STAMP(23);
    }
    do {
      MEMFENCE();
#pragma unroll
      for (int i = 0; i < 16; ++i) qg[i] = ld16<AUX_SC1>(rws, qg_off, i * 1024);
      unsigned bl = 0u, bh = 0u;
#pragma unroll
      for (int i = 0; i < 16; ++i) {
        bl |= (qg[i][0] ^ t8) | (qg[i][2] ^ t8);
        bh |= (qg[i][1] ^ th) | (qg[i][3] ^ th);
      }
      bad = (bl >> 24) | bh;
    } while (__any(bad != 0u) && ++spins < kSpinCap);
#pragma unroll
    for (int i = 0; i < 8; ++i)
      *reinterpret_cast<unsigned*>(lds + L_QIMG + i * 256 + lane * 4) =
          pack_bf16(__uint_as_float(qg[i][0] << 8) + __uint_as_float(qg[8 + i][0] << 8), __uint_as_float(qg[i][2] << 8) + __uint_as_float(qg[8 + i][2] << 8));
    STAMP(3);
    lds_arrive(cnt + C_QIMG, lane);
  }
  PIN();
  LOAD_K(0, 0);
  LOAD_V(0, 0);
  LOAD_K(1, 0);
  LOAD_V(1, 0);
  if (wave != 7) STAMP(3);
  lds_wait<1>(cnt + C_QIMG, 1u);
  const v4u tagv = *reinterpret_cast<const v4u*>(lds + L_TAG);
  const unsigned tag_lo = __builtin_amdgcn_readfirstlane(tagv[2]), tag_hi = __builtin_amdgcn_readfirstlane(tagv[3]);
  STAMP(4);
  const int pp = (lane >> 3) & 1, hh = lane & 7, gg = lane >> 4;
  const v4u Q0 = *reinterpret_cast<const v4u*>(lds + L_QIMG + hh * 256 + pp * 64 + gg * 16);
  const v4u Q1 = *reinterpret_cast<const v4u*>(lds + L_QIMG + hh * 256 + 128 + pp * 64 + gg * 16);

  f32x4 acc[8];
#pragma unroll
  for (int t = 0; t < 8; ++t) acc[t] = f32x4{0.f, 0.f, 0.f, 0.f};
  float m = -INFINITY, l = 0.f;
  const unsigned pmask = pp ? 0xFFFF0000u : 0x0000FFFFu;
#define TILE(buf, AFTER_QK, AFTER_PV)                                                                                \
  do {                                                                                                               \
    float s[4];                                                                                                      \
    _Pragma("unroll") for (int T = 0; T < 4; ++T) {                                                                  \
      f32x4 d = mfma16(Kt[buf][2 * T], Q0, f32x4{0.f, 0.f, 0.f, 0.f});                                               \
      d = mfma16(Kt[buf][2 * T + 1], Q1, d);                                                                         \
      s[T] = (pp ? d[3] : d[0]) + dpp<ROR8>(pp ? d[1] : d[2]);                                                       \
    }                                                                                                                \
    PIN();                                                                                                           \
    AFTER_QK;                                                                                                        \
    float mt = fmaxf(fmaxf(s[0], s[1]), fmaxf(s[2], s[3]));                                                          \
    mt = fmaxf(mt, dpp<ROR8>(mt));                                                                                   \
    mt = swap32_max(swap16_max(mt)) * kC2;                                                                           \
    const float mn = fmaxf(m, mt);                                                                                   \
    if (__any(mn != m)) {                                                                                            \
      const float alpha = __builtin_amdgcn_exp2f(m - mn);                                                            \
      l *= alpha;                                                                                                    \
      _Pragma("unroll") for (int i = 0; i < 8; ++i) acc[i] *= alpha;                                                 \
      m = mn;                                                                                                        \
    }                                                                                                                \
    v4u P;                                                                                                           \
    _Pragma("unroll") for (int T = 0; T < 4; ++T) {                                                                  \
      const float e = __builtin_amdgcn_exp2f(fmaf(s[T], kC2, -m));                                                   \
      l += e;                                                                                                        \
      P[T] = pack_bf16(e, e) & pmask;                                                                                \
    }                                                                                                                \
    _Pragma("unroll") for (int i = 0; i < 8; ++i) acc[i] = mfma16(column_of(Vt[buf], i), P, acc[i]);                 \
    PIN();                                                                                                           \
    AFTER_PV;                                                                                                        \
  } while (0)
  const int nunit = streamer ? kUnitsS : 1;
#pragma unroll 1
  for (int u = 1; u < nunit; ++u) {
    TILE(0, LOAD_K(0, u), LOAD_V(0, u));
    TILE(1, LOAD_K(1, u), LOAD_V(1, u));
  }
  v4u rdy = ld16<AUX_SC1>(rws, streamer ? lane * 16 : kDrop, WS_RDY + br * 1024);
  PIN();
  STAMP(12);
  if (streamer) lds_arrive(cnt + C_LAST, lane);
  PIN();
  TILE(0, PIN(), PIN());
  TILE(1, PIN(), PIN());
#undef TILE
#undef LOAD_K
#undef LOAD_V
  STAMP(13);

  l += dpp<ROR8>(l);
  l = swap16_add(l, l);
  l = swap32_add(l, l);
  {
    float* dst = reinterpret_cast<float*>(lds + L_MRG) + (wave * 8 + hh) * 132 + 32 * gg + 16 * pp;
#pragma unroll
    for (int rr = 0; rr < 2; ++rr)
#pragma unroll
      for (int q = 0; q < 2; ++q) {
        f32x4 o;
#pragma unroll
        for (int e = 0; e < 4; ++e) {
          const f32x4 a = acc[4 * q + e];
          o[e] = (pp ? a[2 + rr] : a[rr]) + dpp<ROR8>(pp ? a[rr] : a[2 + rr]);
        }
        *reinterpret_cast<f32x4*>(dst + 8 * rr + 4 * q) = o;
      }
    if (lane < 8) {
      float* ml = reinterpret_cast<float*>(lds + L_MRGML) + lane * 16;
      ml[wave] = m;
      ml[8 + wave] = l;
    }
  }
  lds_arrive(cnt + C_PART, lane);
  if (streamer) {
    STAMP(7);
    lds_wait(cnt + C_PART, 8u);
    lds_wait(cnt + C_RESET, 4u);
    STAMP(8);
    {
      const int hd = 2 * wave + (lane >> 5);
      const float* ml = reinterpret_cast<const float*>(lds + L_MRGML) + hd * 16;
      const f32x4 m0 = *reinterpret_cast<const f32x4*>(ml), m1 = *reinterpret_cast<const f32x4*>(ml + 4);
      const f32x4 l0 = *reinterpret_cast<const f32x4*>(ml + 8), l1 = *reinterpret_cast<const f32x4*>(ml + 12);
      f32x4 o[8];
#pragma unroll
      for (int w = 0; w < 8; ++w) o[w] = *reinterpret_cast<const f32x4*>(lds + L_MRG + ((w * 8 + hd) * 132 + 4 * (lane & 31)) * 4);
      const float M = fmaxf(fmaxf(fmaxf(m0[0], m0[1]), fmaxf(m0[2], m0[3])), fmaxf(fmaxf(m1[0], m1[1]), fmaxf(m1[2], m1[3])));
      float L = 0.f;
      f32x4 os = {0.f, 0.f, 0.f, 0.f};
#pragma unroll
      for (int w = 0; w < 8; ++w) {
        const float e = __builtin_amdgcn_exp2f((w < 4 ? m0[w & 3] : m1[w & 3]) - M);
        L = fmaf(e, w < 4 ? l0[w & 3] : l1[w & 3], L);
        os += e * o[w];
      }
      const float inv = __builtin_amdgcn_rcpf(L);
      const unsigned u0 = quiet(os[0] * inv), u1 = quiet(os[1] * inv), u2 = quiet(os[2] * inv), u3 = quiet(os[3] * inv);
      const v4u ov = {quiet(M + __builtin_amdgcn_logf(L)), __builtin_amdgcn_perm(u0, u1, 0x07060501u), __builtin_amdgcn_perm(u2, u1, 0x07060302u),
                      __builtin_amdgcn_perm(u3, u2, 0x07060501u)};
      __builtin_amdgcn_raw_buffer_store_b128(ov, rws, WS_PART + wg * kPartSlot + wave * 1024 + lane * 16, 0, AUX_SC1);
    }
    PIN();
    STAMP(14);
#define RDY_BAD() __any(((rdy[0] ^ tag_lo) | (rdy[1] ^ tag_hi) | (rdy[2] ^ tag_lo) | (rdy[3] ^ tag_hi)) != 0u)
    if (RDY_BAD()) {
      unsigned spins = 0;
      do {
        __builtin_amdgcn_s_sleep(2);
        MEMFENCE();
        rdy = ld16<AUX_SC1>(rws, lane * 16, WS_RDY + br * 1024);
      } while (RDY_BAD() && ++spins < kSpinCap);
    }
#undef RDY_BAD
    MEMFENCE();
    const int po = WS_PART + (pair * 32 + (lane >> 1)) * kPartSlot + idx * 128 + 32 * wave + 16 * (lane & 1);
    NAP(K_D1);
    MEMFENCE();
    v4u pd = ld16<AUX_SC1>(rws, po, 0);
    for (unsigned spins = 0;; ++spins) {
      const bool ok = umax4(pd) != kSent;
      if (__all(ok) || spins > kSpinCap) break;
      NAP(K_DP);
      MEMFENCE();
      if (!ok) pd = ld16<AUX_SC1>(rws, po, 0);
    }
    STAMP(15);
    const float ms = __uint_as_float(pd[0]);
    float M = fmaxf(ms, dpp<ROR2>(ms));
    M = fmaxf(M, dpp<ROR4>(M));
    M = fmaxf(M, dpp<ROR8>(M));
    M = swap16_max(M);
    const float e = __builtin_amdgcn_exp2f(ms - M);
    float v[5] = {e, e * __uint_as_float(pd[1] & 0xFFFFFF00u), e * __uint_as_float(__builtin_amdgcn_perm(pd[2], pd[1], 0x0504000Cu)),
                  e * __uint_as_float(__builtin_amdgcn_perm(pd[2], pd[3], 0x0706000Cu)), e * __uint_as_float(pd[3] & 0xFFFFFF00u)};
#pragma unroll
    for (int i = 0; i < 5; ++i) v[i] += dpp<ROR2>(v[i]);
#pragma unroll
    for (int i = 0; i < 5; ++i) v[i] += dpp<ROR4>(v[i]);
#pragma unroll
    for (int i = 0; i < 5; ++i) v[i] += dpp<ROR8>(v[i]);
#pragma unroll
    for (int i = 0; i < 5; ++i) v[i] = swap16_add(v[i], v[i]);
    const float inv = __builtin_amdgcn_rcpf(v[0]);
    const v2u ob = {pack_bf16(v[1] * inv, v[2] * inv), pack_bf16(v[3] * inv, v[4] * inv)};
    if ((lane & 30) == 0) *reinterpret_cast<v2u*>(lds + L_OIMG + (lane >> 5) * 64 + wave * 16 + (lane & 1) * 8) = ob;
    STAMP(10);
    wg_barrier();
    if (wave != 0) { STAMP_FLUSH(); return; }
  } else {
    {
      const v4u s4 = {kSent, kSent, kSent, kSent};
      const int xt = tid - 256;
      if (!K_EARLY_RESET) {
        __builtin_amdgcn_raw_buffer_store_b128(s4, rws, WS_OP + wg * 8192 + xt * 16, 0, AUX_SC1);
        __builtin_amdgcn_raw_buffer_store_b128(s4, rws, WS_OP + wg * 8192 + 4096 + xt * 16, 0, AUX_SC1);
        __builtin_amdgcn_raw_buffer_store_b128(s4, rws, WS_PART + wg * kPartSlot + xt * 16, 0, AUX_SC1);
        asm volatile("s_waitcnt vmcnt(0)" ::: "memory");
        lds_arrive(cnt + C_RESET, lane);
      }
      if (wave == 4) {
        lds_wait<1>(cnt + C_RESET, 4u);
        const v2u t2 = {tag_lo, tag_hi};
        __builtin_amdgcn_raw_buffer_store_b64(t2, rws, lane == 0 ? WS_RDY + wg * 8 : kDrop, 0, AUX_SC1);
      }
    }
    lds_wait<4>(cnt + C_LAST, K_LAST);
    PIN();
    v4u wor[16], wc[16];
    const rsrc_t ro = make_rsrc(p.wo[br] + (size_t)((kvh * 8 + hl) * kDh + rq * 32) * kD, 32 * kD * 2);
#pragma unroll
    for (int i = 0; i < 16; ++i) {
      if ((i & 7) == 0 && K_GATE_WO) GATE_(MEMFENCE());
      wor[i] = ld16<AUX_NT>(ro, gg * 8 * 2048 + (lane & 15) * 16, (i & 7) * 2048 + (i >> 3) * 256 + (wave & 3) * 512);
    }
    PIN();
    STAMP(7);
#pragma unroll
    for (int e = 0; e < 16; ++e) {
      wc[e] = column_of(wor + (e & 8), e & 7);
      asm volatile("" : "+v"(wc[e]));
    }
    STAMP(10);
    wg_barrier();
    STAMP(11);
    const v4u of = *reinterpret_cast<const v4u*>(lds + L_OIMG + (lane & 1) * 64 + gg * 16);
    const int le = (lane & 15) >> 1;
    f32x4 d0 = {0.f, 0.f, 0.f, 0.f}, d1 = {0.f, 0.f, 0.f, 0.f};
#pragma unroll
    for (int e = 0; e < 8; ++e) {
      const v4u z = {0u, 0u, 0u, 0u};
      const v4u bf = le == e ? of : z;
      d0 = mfma16(wc[e], bf, d0);
      d1 = mfma16(wc[8 + e], bf, d1);
    }
    const v4u o0 = {not_sentinel(d0[0]), not_sentinel(d0[1]), not_sentinel(d0[2]), not_sentinel(d0[3])};
    const v4u o1 = {not_sentinel(d1[0]), not_sentinel(d1[1]), not_sentinel(d1[2]), not_sentinel(d1[3])};
    const int so = WS_OP + wg * 8192 + (wave & 3) * 2048 + lane * 16;
    __builtin_amdgcn_raw_buffer_store_b128(o0, rws, so, 0, AUX_SC1);
    __builtin_amdgcn_raw_buffer_store_b128(o1, rws, so + 1024, 0, AUX_SC1);
    STAMP(16);
    STAMP_FLUSH();
    return;
  }

  {
    const int r = wg & 127;
    const int fo = WS_OP + (br * 128 + (lane >> 2)) * 8192 + r * 64 + (lane & 3) * 16;
    v4u f[8];
    NAP(K_D2);
    MEMFENCE();
#pragma unroll
    for (int q = 0; q < 8; ++q) f[q] = ld16<AUX_SC1>(rws, fo, q * 16 * 8192);
    for (unsigned spins = 0;; ++spins) {
      unsigned mx = 0u;
#pragma unroll
      for (int q = 0; q < 8; ++q) mx = max(mx, umax4(f[q]));
      const bool ok = mx != kSent;
      if (__all(ok) || spins > kSpinCap) break;
      NAP(K_DP);
      MEMFENCE();
      if (!ok) {
#pragma unroll
        for (int q = 0; q < 8; ++q) f[q] = ld16<AUX_SC1>(rws, fo, q * 16 * 8192);
      }
    }
    STAMP(17);
    float y[4];
#pragma unroll
    for (int e = 0; e < 4; ++e) {
      y[e] = ((__uint_as_float(f[0][e]) + __uint_as_float(f[1][e])) + (__uint_as_float(f[2][e]) + __uint_as_float(f[3][e]))) +
             ((__uint_as_float(f[4][e]) + __uint_as_float(f[5][e])) + (__uint_as_float(f[6][e]) + __uint_as_float(f[7][e])));
    }
    const float z0 = swap32_add(y[0], y[2]), z1 = swap32_add(y[1], y[3]);
    float z = swap16_add(z0, z1);
    z += dpp<ROR4>(z);
    z += dpp<ROR8>(z);
    if ((lane & 12) == 0) {
      const int pos = 4 * r + (lane & 3), ln = pos & 63, rr = lane >> 4;
      const int col = 128 * (pos >> 6) + 32 * (ln >> 4) + 8 * rr + ((ln & 15) >> 1);
      p.out[(ln & 1) * 2048 + br * 1024 + col] = (unsigned short)(pack_bf16(z, z) & 0xFFFFu);
    }
    if (r == 0 && lane == 0) {
      const u64 nx = (((u64)tagv[1] << 32) | tagv[0]) + 1;
      const v2u e2 = {(unsigned)nx, (unsigned)(nx >> 32)};
      __builtin_amdgcn_raw_buffer_store_b64(e2, rws, WS_EPOCH + br * 256, 0, AUX_SC1);
    }
    STAMP(18);
    STAMP_FLUSH();
  }
}

}

extern "C" void kernel_launch(void* const* d_in, const int* in_sizes, int n_in, void* d_out, int out_size, void* d_ws,
                              size_t ws_size, hipStream_t stream) {
  (void)in_sizes; (void)n_in; (void)out_size; (void)ws_size;
  Params p;
  for (int i = 0; i < 2; ++i) {
    p.x[i] = (const unsigned short*)d_in[5 * i + 0];
    p.k[i] = (const unsigned short*)d_in[5 * i + 1];
    p.v[i] = (const unsigned short*)d_in[5 * i + 2];
    p.wq[i] = (const unsigned short*)d_in[5 * i + 3];
    p.wo[i] = (const unsigned short*)d_in[5 * i + 4];
  }
  p.out = (unsigned short*)d_out;
  p.ws = (unsigned char*)d_ws;
  hipLaunchKernelGGL(fused_decode, dim3(kNWG), dim3(kNThr), 0, stream, p);
}
```

```cpp
#define POLICY 8
#include <hip/hip_runtime.h>
#include <hip/hip_bf16.h>
#include <stdint.h>

typedef unsigned v4u __attribute__((ext_vector_type(4)));
typedef unsigned v2u __attribute__((ext_vector_type(2)));
typedef float f32x4 __attribute__((ext_vector_type(4)));
typedef __bf16 bf16x8 __attribute__((ext_vector_type(8)));
typedef __bf16 bf16x2 __attribute__((ext_vector_type(2)));
typedef unsigned long long u64;
typedef __amdgpu_buffer_rsrc_t rsrc_t;

namespace {

constexpr int kD = 1024, kHkv = 4, kSk = 32768, kDh = 128;
constexpr int kNWG = 256, kNThr = 512;
constexpr int kUnitsS = 7;
#define SCHED_T0 400
#define SCHED_TAU 469
constexpr int AUX_NT = 2, AUX_SC1 = 16;
constexpr int RSRC_W3 = 0x00020000;
constexpr unsigned kSent = 0xFFFFFFFFu;
constexpr int kDrop = 0x7FFFFF00;
constexpr unsigned kSpinCap = 1u << 18;
constexpr float kC2 = 0.088388347648318440f * 1.4426950408889634f;

constexpr int WS_EPOCH = 0;
constexpr int WS_RDY = 1024;
constexpr int WS_QG = 4096;
constexpr int kPartSlot = 4096;
constexpr int WS_PART = WS_QG + 8 * 2 * 2 * 1024 * 8;
constexpr int WS_OP = WS_PART + 256 * kPartSlot;
constexpr int WS_END = WS_OP + 256 * 8192;

constexpr int L_X = 0;
constexpr int L_TAG = L_X + 2048;
constexpr int L_CNT = L_TAG + 16;
constexpr int L_QRED = L_CNT + 32;
constexpr int L_QIMG = L_QRED + 4 * 512;
constexpr int L_OIMG = L_QIMG + 2048;
constexpr int L_MRGML = L_OIMG + 128;
constexpr int L_MRG = L_MRGML + 8 * 16 * 4;
constexpr int L_END = L_MRG + 8 * 8 * 132 * 4;

enum { C_QPART, C_QIMG, C_RESET, C_LAST, C_PART, C_TICKET, C_T0 };

struct Params {
  const unsigned short* x[2];
  const unsigned short* k[2];
  const unsigned short* v[2];
  const unsigned short* wq[2];
  const unsigned short* wo[2];
  unsigned short* out;
  unsigned char* ws;
};

__device__ __forceinline__ rsrc_t make_rsrc(const void* p, int bytes) {
  return __builtin_amdgcn_make_buffer_rsrc((void*)p, 0, bytes, RSRC_W3);
}
template <int AUX> __device__ __forceinline__ v4u ld16(rsrc_t r, int voff, int soff) {
  return __builtin_amdgcn_raw_buffer_load_b128(r, voff, soff, AUX);
}
__device__ __forceinline__ f32x4 mfma16(v4u a, v4u b, f32x4 c) {
  return __builtin_amdgcn_mfma_f32_16x16x32_bf16(__builtin_bit_cast(bf16x8, a), __builtin_bit_cast(bf16x8, b), c, 0, 0, 0);
}
__device__ __forceinline__ float dot2(unsigned a, unsigned b, float c) {
  return __builtin_amdgcn_fdot2_f32_bf16(__builtin_bit_cast(bf16x2, a), __builtin_bit_cast(bf16x2, b), c, false);
}
__device__ __forceinline__ unsigned pack_bf16(float lo, float hi) {
  bf16x2 r = {(__bf16)lo, (__bf16)hi};
  return __builtin_bit_cast(unsigned, r);
}
template <int CTRL> __device__ __forceinline__ float dpp(float x) {
  return __builtin_bit_cast(float, __builtin_amdgcn_mov_dpp(__builtin_bit_cast(int, x), CTRL, 0xf, 0xf, true));
}
constexpr int ROR2 = 0x122, ROR4 = 0x124, ROR8 = 0x128;
__device__ __forceinline__ float swap16_add(float a, float b) {
  auto s = __builtin_amdgcn_permlane16_swap(__float_as_uint(a), __float_as_uint(b), false, false);
  return __uint_as_float(s[0]) + __uint_as_float(s[1]);
}
__device__ __forceinline__ float swap32_add(float a, float b) {
  auto s = __builtin_amdgcn_permlane32_swap(__float_as_uint(a), __float_as_uint(b), false, false);
  return __uint_as_float(s[0]) + __uint_as_float(s[1]);
}
__device__ __forceinline__ float swap16_max(float a) {
  auto s = __builtin_amdgcn_permlane16_swap(__float_as_uint(a), __float_as_uint(a), false, false);
  return fmaxf(__uint_as_float(s[0]), __uint_as_float(s[1]));
}
__device__ __forceinline__ float swap32_max(float a) {
  auto s = __builtin_amdgcn_permlane32_swap(__float_as_uint(a), __float_as_uint(a), false, false);
  return fmaxf(__uint_as_float(s[0]), __uint_as_float(s[1]));
}
__device__ __forceinline__ unsigned perm_lo(unsigned hi, unsigned lo) { return __builtin_amdgcn_perm(hi, lo, 0x05040100u); }
__device__ __forceinline__ unsigned perm_hi(unsigned hi, unsigned lo) { return __builtin_amdgcn_perm(hi, lo, 0x07060302u); }
__device__ __forceinline__ v4u column_of(const v4u* r, int t) {
  v4u o;
#pragma unroll
  for (int u = 0; u < 4; ++u)
    o[u] = (t & 1) ? perm_hi(r[2 * u + 1][t >> 1], r[2 * u][t >> 1]) : perm_lo(r[2 * u + 1][t >> 1], r[2 * u][t >> 1]);
  return o;
}
__device__ __forceinline__ unsigned not_sentinel(float f) {
  const unsigned u = __float_as_uint(f);
  return u == kSent ? 0x7FC00000u : u;
}
__device__ __forceinline__ unsigned quiet(float f) { return f != f ? 0x7FC00000u : __float_as_uint(f); }
__device__ __forceinline__ unsigned umax4(v4u a) { return max(max(a[0], a[1]), max(a[2], a[3])); }
__device__ __forceinline__ u64 mix64(u64 z) {
  z += 0x9E3779B97F4A7C15ull;
  z = (z ^ (z >> 30)) * 0xBF58476D1CE4E5B9ull;
  z = (z ^ (z >> 27)) * 0x94D049BB133111EBull;
  return z ^ (z >> 31);
}
#define STAMP(i) do {} while (0)
#define STAMP_FLUSH() do {} while (0)
#define PIN() __builtin_amdgcn_sched_barrier(0)
#define MEMFENCE() asm volatile("" ::: "memory")
__device__ __forceinline__ void wg_barrier() {
  asm volatile("s_waitcnt lgkmcnt(0)" ::: "memory");
  __builtin_amdgcn_s_barrier();
  asm volatile("" ::: "memory");
}

__device__ __forceinline__ void lds_arrive(unsigned* c, int lane) {
  asm volatile("s_waitcnt lgkmcnt(0)" ::: "memory");
  if (lane == 0) __hip_atomic_fetch_add(c, 1u, __ATOMIC_RELAXED, __HIP_MEMORY_SCOPE_WORKGROUP);
}
template <int SLEEP = 0> __device__ __forceinline__ void lds_wait(unsigned* c, unsigned n) {
  while (__hip_atomic_load(c, __ATOMIC_RELAXED, __HIP_MEMORY_SCOPE_WORKGROUP) < n) __builtin_amdgcn_s_sleep(SLEEP);
  MEMFENCE();
}

__global__ void __launch_bounds__(kNThr, 2) fused_decode(Params p) {
  __shared__ __attribute__((aligned(16))) unsigned char lds[L_END];
  const int tid = threadIdx.x, lane = tid & 63;
  const int wave = __builtin_amdgcn_readfirstlane(tid >> 6);
  const bool streamer = wave < 4;
  const int wg = blockIdx.x, pair = wg >> 5, idx = wg & 31;
  const int br = pair >> 2, kvh = pair & 3, b = idx >> 4, split = idx & 15;
  const int qblk = idx & 15, kh = idx >> 4;
  const int hl = idx >> 2, rq = idx & 3;
  STAMP(0);
  const unsigned t_in = (unsigned)__builtin_amdgcn_s_memrealtime();
  PIN();
  const rsrc_t rws = make_rsrc(p.ws, WS_END);
  const size_t kv_off = ((size_t)(b * kHkv + kvh) * kSk + (size_t)(split * 64 + (streamer ? wave : 8 * kUnitsS - 4 + wave)) * 32) * kDh;
  const rsrc_t rk = make_rsrc(p.k[br] + kv_off, streamer ? (2 * kUnitsS - 1) * 32768 + 8192 : 32768 + 8192);
  const rsrc_t rv = make_rsrc(p.v[br] + kv_off, streamer ? (2 * kUnitsS - 1) * 32768 + 8192 : 32768 + 8192);

  unsigned* const cnt = reinterpret_cast<unsigned*>(lds + L_CNT);
  v4u wqr[16], xv;
  v2u ep;
  if (!streamer) {
    const int xw = wave - 4;
    const rsrc_t rq_ = make_rsrc(p.wq[br] + (size_t)(kh * 512 + xw * 128) * 4096 + kvh * 1024 + qblk * 64, 127 * 8192 + 128);
    const rsrc_t rx = make_rsrc(p.x[br] + kh * 512 + xw * 128, 2048 + 256);
    const int qo = (lane >> 3) * 16 * 8192 + (lane & 7) * 16;
    ep = __builtin_amdgcn_raw_buffer_load_b64(rws, wave == 7 ? WS_EPOCH + br * 256 : kDrop, 0, AUX_SC1);
    xv = ld16<0>(rx, lane < 32 ? (lane >> 4) * 2048 + (lane & 15) * 16 : kDrop, 0);
#pragma unroll
    for (int i = 0; i < 16; ++i) wqr[i] = ld16<AUX_NT>(rq_, qo, i * 8192);
    if (tid < 256 + 8) cnt[lane] = lane == C_T0 ? t_in : 0u;
  }
  PIN();
  wg_barrier();
  PIN();
  const int kl = (lane & 15) >> 1;
  int koff = (4 * (kl & 1) + (kl >> 1)) * 256 + (4 * (lane & 1) + (lane >> 4)) * 16;
  int voff = lane * 16;
  v4u Kt[2][8], Vt[2][8];
#define K_T0 SCHED_T0
#define K_TAU SCHED_TAU
#define K_GATE_WO 1
#define K_LAST 4u
#define GROUP_CAP() asm volatile("s_waitcnt vmcnt(4)" ::: "memory")
#define K_LIGHT 0
#define K_EARLY_RESET 1
#define K_TOUCH 1
#define NAP(n) do { for (int z_ = 0; z_ < (n); ++z_) __builtin_amdgcn_s_sleep(2); } while (0)
#define K_D1 12
#define K_D2 20
#define K_DP 4
  const unsigned t_gate = __builtin_amdgcn_readfirstlane(cnt[C_T0]) + K_T0;
#define GATE_(CAP)                                                                                                   \
  do {                                                                                                               \
    unsigned tk_ = 0;                                                                                                \
    if (lane == 0) tk_ = __hip_atomic_fetch_add(cnt + C_TICKET, 1u, __ATOMIC_RELAXED, __HIP_MEMORY_SCOPE_WORKGROUP); \
    const unsigned due_ = t_gate + ((__builtin_amdgcn_readfirstlane(tk_) * K_TAU) >> 4);                             \
    CAP;                                                                                                             \
    while ((int)((unsigned)__builtin_amdgcn_s_memrealtime() - due_) < 0) __builtin_amdgcn_s_sleep(1);               \
    MEMFENCE();                                                                                                      \
  } while (0)
#define GATE() GATE_(GROUP_CAP())
  const bool KEEP_K = (POLICY >> (2 * br)) & 1, KEEP_V = (POLICY >> (2 * br + 1)) & 1;
#define LOAD_K(buf, u)                                                                                               \
  do {                                                                                                               \
    GATE();                                                                                                          \
    asm volatile("" : "+v"(koff));                                                                                   \
    if (KEEP_K) {                                                                                                    \
      _Pragma("unroll") for (int i = 0; i < 8; ++i)                                                                  \
          Kt[buf][i] = ld16<0>(rk, koff + (i & 2) * 1024 + (i & 1) * 128, (u) * 65536 + (buf) * 32768 + (i >> 2) * 4096); \
    } else {                                                                                                         \
      _Pragma("unroll") for (int i = 0; i < 8; ++i)                                                                  \
          Kt[buf][i] = ld16<AUX_NT>(rk, koff + (i & 2) * 1024 + (i & 1) * 128, (u) * 65536 + (buf) * 32768 + (i >> 2) * 4096); \
    }                                                                                                                \
    PIN();                                                                                                           \
  } while (0)
#define LOAD_V(buf, u)                                                                                               \
  do {                                                                                                               \
    GATE();                                                                                                          \
    asm volatile("" : "+v"(voff));                                                                                   \
    if (KEEP_V) {                                                                                                    \
      _Pragma("unroll") for (int i = 0; i < 8; ++i)                                                                  \
          Vt[buf][i] = ld16<0>(rv, voff + (i & 3) * 1024, (u) * 65536 + (buf) * 32768 + (i >> 2) * 4096);            \
    } else {                                                                                                         \
      _Pragma("unroll") for (int i = 0; i < 8; ++i)                                                                  \
          Vt[buf][i] = ld16<AUX_NT>(rv, voff + (i & 3) * 1024, (u) * 65536 + (buf) * 32768 + (i >> 2) * 4096);       \
    }                                                                                                                \
    PIN();                                                                                                           \
  } while (0)
  if (streamer) {
    if (K_TOUCH) {
      const rsrc_t ro_ = make_rsrc(p.wo[br] + (size_t)((kvh * 8 + hl) * kDh + rq * 32) * kD, 32 * kD * 2);
      const int to = lane == 0 ? 0 : kDrop;
      unsigned t0_ = __builtin_amdgcn_raw_buffer_load_b32(rk, to, wave * 98304, AUX_NT);
      unsigned t1_ = __builtin_amdgcn_raw_buffer_load_b32(rv, to, wave * 98304, AUX_NT);
      unsigned t2_ = __builtin_amdgcn_raw_buffer_load_b32(ro_, to, wave * 16384, AUX_NT);
      asm volatile("" ::"v"(t0_), "v"(t1_), "v"(t2_));
    }
    if (K_EARLY_RESET) {
      const v4u s4 = {kSent, kSent, kSent, kSent};
      __builtin_amdgcn_raw_buffer_store_b128(s4, rws, WS_OP + wg * 8192 + tid * 16, 0, AUX_SC1);
      __builtin_amdgcn_raw_buffer_store_b128(s4, rws, WS_OP + wg * 8192 + 4096 + tid * 16, 0, AUX_SC1);
      __builtin_amdgcn_raw_buffer_store_b128(s4, rws, WS_PART + wg * kPartSlot + tid * 16, 0, AUX_SC1);
      asm volatile("s_waitcnt vmcnt(0)" ::: "memory");
      lds_arrive(cnt + C_RESET, lane);
    }
  } else {
    const int xw = wave - 4;
    if (lane < 32) *reinterpret_cast<v4u*>(lds + L_X + xw * 512 + lane * 16) = xv;
    asm volatile("s_waitcnt lgkmcnt(0)" ::: "memory");
    float qa[16];
#pragma unroll
    for (int i = 0; i < 16; ++i) qa[i] = 0.f;
#pragma unroll
    for (int h = 0; h < 2; ++h) {
      const v4u x0 = *reinterpret_cast<const v4u*>(lds + L_X + xw * 512 + (lane >> 3) * 32 + h * 16);
      const v4u x1 = *reinterpret_cast<const v4u*>(lds + L_X + xw * 512 + 256 + (lane >> 3) * 32 + h * 16);
#pragma unroll
      for (int e = 0; e < 8; ++e) {
        const v4u c = column_of(wqr + 8 * h, e);
#pragma unroll
        for (int u = 0; u < 4; ++u) {
          qa[e] = dot2(c[u], x0[u], qa[e]);
          qa[8 + e] = dot2(c[u], x1[u], qa[8 + e]);
        }
      }
    }
    float r8[8], r4[4];
#pragma unroll
    for (int e = 0; e < 8; ++e) r8[e] = swap32_add(qa[e], qa[8 + e]);
#pragma unroll
    for (int e = 0; e < 4; ++e) r4[e] = swap16_add(r8[e], r8[4 + e]);
    const bool up = lane & 8;
    const float o0 = (up ? r4[2] : r4[0]) + dpp<ROR8>(up ? r4[0] : r4[2]);
    const float o1 = (up ? r4[3] : r4[1]) + dpp<ROR8>(up ? r4[1] : r4[3]);
    *reinterpret_cast<float2*>(lds + L_QRED + xw * 512 + lane * 8) = make_float2(o0, o1);
    STAMP(1);
    lds_arrive(cnt + C_QPART, lane);
  }
  if (wave == 7) {
    const u64 epoch = ((u64)(unsigned)__builtin_amdgcn_readfirstlane(ep[1]) << 32) | (unsigned)__builtin_amdgcn_readfirstlane(ep[0]);
    const u64 tg = mix64(epoch);
    const unsigned t8 = (unsigned)tg & 0xFF000000u, th = (unsigned)(tg >> 32);
    if (lane == 0) *reinterpret_cast<v4u*>(lds + L_TAG) = v4u{(unsigned)epoch, (unsigned)(epoch >> 32), (unsigned)tg, th};
    lds_wait(cnt + C_QPART, 4u);
    float s0 = 0.f, s1 = 0.f;
#pragma unroll
    for (int w = 0; w < 4; ++w) {
      const float2 q = *reinterpret_cast<const float2*>(lds + L_QRED + w * 512 + lane * 8);
      s0 += q.x;
      s1 += q.y;
    }
    const v4u g = {t8 | (__float_as_uint(s0) >> 8), th, t8 | (__float_as_uint(s1) >> 8), th};
    const int col = 8 * (lane & 7) + 4 * ((lane >> 4) & 1) + 2 * ((lane >> 3) & 1);
    __builtin_amdgcn_raw_buffer_store_b128(g, rws, WS_QG + (((pair * 2 + (lane >> 5)) * 2 + kh) * 1024 + qblk * 64 + col) * 8, 0, AUX_SC1);
    STAMP(2);
    const int qg_off = WS_QG + (pair * 2 + b) * 16384 + lane * 16;
    v4u qg[16];
    unsigned spins = 0;
    unsigned bad;
    if (K_LIGHT) {
      const int lo = lane < 32 ? WS_QG + (pair * 2 + b) * 16384 + lane * 512 : kDrop;
      do {
        MEMFENCE();
        const v2u g1 = __builtin_amdgcn_raw_buffer_load_b64(rws, lo, 0, AUX_SC1);
        bad = lane < 32 ? ((g1[0] ^ t8) >> 24) | (g1[1] ^ th) : 0u;
      } while (__any(bad != 0u) && ++spins < kSpinCap);
      STAMP(23);
    }
    do {
      MEMFENCE();
#pragma unroll
      for (int i = 0; i < 16; ++i) qg[i] = ld16<AUX_SC1>(rws, qg_off, i * 1024);
      unsigned bl = 0u, bh = 0u;
#pragma unroll
      for (int i = 0; i < 16; ++i) {
        bl |= (qg[i][0] ^ t8) | (qg[i][2] ^ t8);
        bh |= (qg[i][1] ^ th) | (qg[i][3] ^ th);
      }
      bad = (bl >> 24) | bh;
    } while (__any(bad != 0u) && ++spins < kSpinCap);
#pragma unroll
    for (int i = 0; i < 8; ++i)
      *reinterpret_cast<unsigned*>(lds + L_QIMG + i * 256 + lane * 4) =
          pack_bf16(__uint_as_float(qg[i][0] << 8) + __uint_as_float(qg[8 + i][0] << 8), __uint_as_float(qg[i][2] << 8) + __uint_as_float(qg[8 + i][2] << 8));
    STAMP(3);
    lds_arrive(cnt + C_QIMG, lane);
  }
  PIN();
  LOAD_K(0, 0);
  LOAD_V(0, 0);
  LOAD_K(1, 0);
  LOAD_V(1, 0);
  if (wave != 7) STAMP(3);
  lds_wait<1>(cnt + C_QIMG, 1u);
  const v4u tagv = *reinterpret_cast<const v4u*>(lds + L_TAG);
  const unsigned tag_lo = __builtin_amdgcn_readfirstlane(tagv[2]), tag_hi = __builtin_amdgcn_readfirstlane(tagv[3]);
  STAMP(4);
  const int pp = (lane >> 3) & 1, hh = lane & 7, gg = lane >> 4;
  const v4u Q0 = *reinterpret_cast<const v4u*>(lds + L_QIMG + hh * 256 + pp * 64 + gg * 16);
  const v4u Q1 = *reinterpret_cast<const v4u*>(lds + L_QIMG + hh * 256 + 128 + pp * 64 + gg * 16);

  f32x4 acc[8];
#pragma unroll
  for (int t = 0; t < 8; ++t) acc[t] = f32x4{0.f, 0.f, 0.f, 0.f};
  float m = -INFINITY, l = 0.f;
  const unsigned pmask = pp ? 0xFFFF0000u : 0x0000FFFFu;
#define TILE(buf, AFTER_QK, AFTER_PV)                                                                                \
  do {                                                                                                               \
    float s[4];                                                                                                      \
    _Pragma("unroll") for (int T = 0; T < 4; ++T) {                                                                  \
      f32x4 d = mfma16(Kt[buf][2 * T], Q0, f32x4{0.f, 0.f, 0.f, 0.f});                                               \
      d = mfma16(Kt[buf][2 * T + 1], Q1, d);                                                                         \
      s[T] = (pp ? d[3] : d[0]) + dpp<ROR8>(pp ? d[1] : d[2]);                                                       \
    }                                                                                                                \
    PIN();                                                                                                           \
    AFTER_QK;                                                                                                        \
    float mt = fmaxf(fmaxf(s[0], s[1]), fmaxf(s[2], s[3]));                                                          \
    mt = fmaxf(mt, dpp<ROR8>(mt));                                                                                   \
    mt = swap32_max(swap16_max(mt)) * kC2;                                                                           \
    const float mn = fmaxf(m, mt);                                                                                   \
    if (__any(mn != m)) {                                                                                            \
      const float alpha = __builtin_amdgcn_exp2f(m - mn);                                                            \
      l *= alpha;                                                                                                    \
      _Pragma("unroll") for (int i = 0; i < 8; ++i) acc[i] *= alpha;                                                 \
      m = mn;                                                                                                        \
    }                                                                                                                \
    v4u P;                                                                                                           \
    _Pragma("unroll") for (int T = 0; T < 4; ++T) {                                                                  \
      const float e = __builtin_amdgcn_exp2f(fmaf(s[T], kC2, -m));                                                   \
      l += e;                                                                                                        \
      P[T] = pack_bf16(e, e) & pmask;                                                                                \
    }                                                                                                                \
    _Pragma("unroll") for (int i = 0; i < 8; ++i) acc[i] = mfma16(column_of(Vt[buf], i), P, acc[i]);                 \
    PIN();                                                                                                           \
    AFTER_PV;                                                                                                        \
  } while (0)
  const int nunit = streamer ? kUnitsS : 1;
#pragma unroll 1
  for (int u = 1; u < nunit; ++u) {
    TILE(0, LOAD_K(0, u), LOAD_V(0, u));
    TILE(1, LOAD_K(1, u), LOAD_V(1, u));
  }
  v4u rdy = ld16<AUX_SC1>(rws, streamer ? lane * 16 : kDrop, WS_RDY + br * 1024);
  PIN();
  STAMP(12);
  if (streamer) lds_arrive(cnt + C_LAST, lane);
  PIN();
  TILE(0, PIN(), PIN());
  TILE(1, PIN(), PIN());
#undef TILE
#undef LOAD_K
#undef LOAD_V
  STAMP(13);

  l += dpp<ROR8>(l);
  l = swap16_add(l, l);
  l = swap32_add(l, l);
  {
    float* dst = reinterpret_cast<float*>(lds + L_MRG) + (wave * 8 + hh) * 132 + 32 * gg + 16 * pp;
#pragma unroll
    for (int rr = 0; rr < 2; ++rr)
#pragma unroll
      for (int q = 0; q < 2; ++q) {
        f32x4 o;
#pragma unroll
        for (int e = 0; e < 4; ++e) {
          const f32x4 a = acc[4 * q + e];
          o[e] = (pp ? a[2 + rr] : a[rr]) + dpp<ROR8>(pp ? a[rr] : a[2 + rr]);
        }
        *reinterpret_cast<f32x4*>(dst + 8 * rr + 4 * q) = o;
      }
    if (lane < 8) {
      float* ml = reinterpret_cast<float*>(lds + L_MRGML) + lane * 16;
      ml[wave] = m;
      ml[8 + wave] = l;
    }
  }
  lds_arrive(cnt + C_PART, lane);
  if (streamer) {
    STAMP(7);
    lds_wait(cnt + C_PART, 8u);
    lds_wait(cnt + C_RESET, 4u);
    STAMP(8);
    {
      const int hd = 2 * wave + (lane >> 5);
      const float* ml = reinterpret_cast<const float*>(lds + L_MRGML) + hd * 16;
      const f32x4 m0 = *reinterpret_cast<const f32x4*>(ml), m1 = *reinterpret_cast<const f32x4*>(ml + 4);
      const f32x4 l0 = *reinterpret_cast<const f32x4*>(ml + 8), l1 = *reinterpret_cast<const f32x4*>(ml + 12);
      f32x4 o[8];
#pragma unroll
      for (int w = 0; w < 8; ++w) o[w] = *reinterpret_cast<const f32x4*>(lds + L_MRG + ((w * 8 + hd) * 132 + 4 * (lane & 31)) * 4);
      const float M = fmaxf(fmaxf(fmaxf(m0[0], m0[1]), fmaxf(m0[2], m0[3])), fmaxf(fmaxf(m1[0], m1[1]), fmaxf(m1[2], m1[3])));
      float L = 0.f;
      f32x4 os = {0.f, 0.f, 0.f, 0.f};
#pragma unroll
      for (int w = 0; w < 8; ++w) {
        const float e = __builtin_amdgcn_exp2f((w < 4 ? m0[w & 3] : m1[w & 3]) - M);
        L = fmaf(e, w < 4 ? l0[w & 3] : l1[w & 3], L);
        os += e * o[w];
      }
      const float inv = __builtin_amdgcn_rcpf(L);
      const unsigned u0 = quiet(os[0] * inv), u1 = quiet(os[1] * inv), u2 = quiet(os[2] * inv), u3 = quiet(os[3] * inv);
      const v4u ov = {quiet(M + __builtin_amdgcn_logf(L)), __builtin_amdgcn_perm(u0, u1, 0x07060501u), __builtin_amdgcn_perm(u2, u1, 0x07060302u),
                      __builtin_amdgcn_perm(u3, u2, 0x07060501u)};
      __builtin_amdgcn_raw_buffer_store_b128(ov, rws, WS_PART + wg * kPartSlot + wave * 1024 + lane * 16, 0, AUX_SC1);
    }
    PIN();
    STAMP(14);
#define RDY_BAD() __any(((rdy[0] ^ tag_lo) | (rdy[1] ^ tag_hi) | (rdy[2] ^ tag_lo) | (rdy[3] ^ tag_hi)) != 0u)
    if (RDY_BAD()) {
      unsigned spins = 0;
      do {
        __builtin_amdgcn_s_sleep(2);
        MEMFENCE();
        rdy = ld16<AUX_SC1>(rws, lane * 16, WS_RDY + br * 1024);
      } while (RDY_BAD() && ++spins < kSpinCap);
    }
#undef RDY_BAD
    MEMFENCE();
    const int po = WS_PART + (pair * 32 + (lane >> 1)) * kPartSlot + idx * 128 + 32 * wave + 16 * (lane & 1);
    NAP(K_D1);
    MEMFENCE();
    v4u pd = ld16<AUX_SC1>(rws, po, 0);
    for (unsigned spins = 0;; ++spins) {
      const bool ok = umax4(pd) != kSent;
      if (__all(ok) || spins > kSpinCap) break;
      NAP(K_DP);
      MEMFENCE();
      if (!ok) pd = ld16<AUX_SC1>(rws, po, 0);
    }
    STAMP(15);
    const float ms = __uint_as_float(pd[0]);
    float M = fmaxf(ms, dpp<ROR2>(ms));
    M = fmaxf(M, dpp<ROR4>(M));
    M = fmaxf(M, dpp<ROR8>(M));
    M = swap16_max(M);
    const float e = __builtin_amdgcn_exp2f(ms - M);
    float v[5] = {e, e * __uint_as_float(pd[1] & 0xFFFFFF00u), e * __uint_as_float(__builtin_amdgcn_perm(pd[2], pd[1], 0x0504000Cu)),
                  e * __uint_as_float(__builtin_amdgcn_perm(pd[2], pd[3], 0x0706000Cu)), e * __uint_as_float(pd[3] & 0xFFFFFF00u)};
#pragma unroll
    for (int i = 0; i < 5; ++i) v[i] += dpp<ROR2>(v[i]);
#pragma unroll
    for (int i = 0; i < 5; ++i) v[i] += dpp<ROR4>(v[i]);
#pragma unroll
    for (int i = 0; i < 5; ++i) v[i] += dpp<ROR8>(v[i]);
#pragma unroll
    for (int i = 0; i < 5; ++i) v[i] = swap16_add(v[i], v[i]);
    const float inv = __builtin_amdgcn_rcpf(v[0]);
    const v2u ob = {pack_bf16(v[1] * inv, v[2] * inv), pack_bf16(v[3] * inv, v[4] * inv)};
    if ((lane & 30) == 0) *reinterpret_cast<v2u*>(lds + L_OIMG + (lane >> 5) * 64 + wave * 16 + (lane & 1) * 8) = ob;
    STAMP(10);
    wg_barrier();
    if (wave != 0) { STAMP_FLUSH(); return; }
  } else {
    {
      const v4u s4 = {kSent, kSent, kSent, kSent};
      const int xt = tid - 256;
      if (!K_EARLY_RESET) {
        __builtin_amdgcn_raw_buffer_store_b128(s4, rws, WS_OP + wg * 8192 + xt * 16, 0, AUX_SC1);
        __builtin_amdgcn_raw_buffer_store_b128(s4, rws, WS_OP + wg * 8192 + 4096 + xt * 16, 0, AUX_SC1);
        __builtin_amdgcn_raw_buffer_store_b128(s4, rws, WS_PART + wg * kPartSlot + xt * 16, 0, AUX_SC1);
        asm volatile("s_waitcnt vmcnt(0)" ::: "memory");
        lds_arrive(cnt + C_RESET, lane);
      }
      if (wave == 4) {
        lds_wait<1>(cnt + C_RESET, 4u);
        const v2u t2 = {tag_lo, tag_hi};
        __builtin_amdgcn_raw_buffer_store_b64(t2, rws, lane == 0 ? WS_RDY + wg * 8 : kDrop, 0, AUX_SC1);
      }
    }
    lds_wait<4>(cnt + C_LAST, K_LAST);
    PIN();
    v4u wor[16], wc[16];
    const rsrc_t ro = make_rsrc(p.wo[br] + (size_t)((kvh * 8 + hl) * kDh + rq * 32) * kD, 32 * kD * 2);
#pragma unroll
    for (int i = 0; i < 16; ++i) {
      if ((i & 7) == 0 && K_GATE_WO) GATE_(MEMFENCE());
      wor[i] = ld16<AUX_NT>(ro, gg * 8 * 2048 + (lane & 15) * 16, (i & 7) * 2048 + (i >> 3) * 256 + (wave & 3) * 512);
    }
    PIN();
    STAMP(7);
#pragma unroll
    for (int e = 0; e < 16; ++e) {
      wc[e] = column_of(wor + (e & 8), e & 7);
      asm volatile("" : "+v"(wc[e]));
    }
    STAMP(10);
    wg_barrier();
    STAMP(11);
    const v4u of = *reinterpret_cast<const v4u*>(lds + L_OIMG + (lane & 1) * 64 + gg * 16);
    const int le = (lane & 15) >> 1;
    f32x4 d0 = {0.f, 0.f, 0.f, 0.f}, d1 = {0.f, 0.f, 0.f, 0.f};
#pragma unroll
    for (int e = 0; e < 8; ++e) {
      const v4u z = {0u, 0u, 0u, 0u};
      const v4u bf = le == e ? of : z;
      d0 = mfma16(wc[e], bf, d0);
      d1 = mfma16(wc[8 + e], bf, d1);
    }
    const v4u o0 = {not_sentinel(d0[0]), not_sentinel(d0[1]), not_sentinel(d0[2]), not_sentinel(d0[3])};
    const v4u o1 = {not_sentinel(d1[0]), not_sentinel(d1[1]), not_sentinel(d1[2]), not_sentinel(d1[3])};
    const int so = WS_OP + wg * 8192 + (wave & 3) * 2048 + lane * 16;
    __builtin_amdgcn_raw_buffer_store_b128(o0, rws, so, 0, AUX_SC1);
    __builtin_amdgcn_raw_buffer_store_b128(o1, rws, so + 1024, 0, AUX_SC1);
    STAMP(16);
    STAMP_FLUSH();
    return;
  }

  {
    const int r = wg & 127;
    const int fo = WS_OP + (br * 128 + (lane >> 2)) * 8192 + r * 64 + (lane & 3) * 16;
    v4u f[8];
    NAP(K_D2);
    MEMFENCE();
#pragma unroll
    for (int q = 0; q < 8; ++q) f[q] = ld16<AUX_SC1>(rws, fo, q * 16 * 8192);
    for (unsigned spins = 0;; ++spins) {
      unsigned mx = 0u;
#pragma unroll
      for (int q = 0; q < 8; ++q) mx = max(mx, umax4(f[q]));
      const bool ok = mx != kSent;
      if (__all(ok) || spins > kSpinCap) break;
      NAP(K_DP);
      MEMFENCE();
      if (!ok) {
#pragma unroll
        for (int q = 0; q < 8; ++q) f[q] = ld16<AUX_SC1>(rws, fo, q * 16 * 8192);
      }
    }
    STAMP(17);
    float y[4];
#pragma unroll
    for (int e = 0; e < 4; ++e) {
      y[e] = ((__uint_as_float(f[0][e]) + __uint_as_float(f[1][e])) + (__uint_as_float(f[2][e]) + __uint_as_float(f[3][e]))) +
             ((__uint_as_float(f[4][e]) + __uint_as_float(f[5][e])) + (__uint_as_float(f[6][e]) + __uint_as_float(f[7][e])));
    }
    const float z0 = swap32_add(y[0], y[2]), z1 = swap32_add(y[1], y[3]);
    float z = swap16_add(z0, z1);
    z += dpp<ROR4>(z);
    z += dpp<ROR8>(z);
    if ((lane & 12) == 0) {
      const int pos = 4 * r + (lane & 3), ln = pos & 63, rr = lane >> 4;
      const int col = 128 * (pos >> 6) + 32 * (ln >> 4) + 8 * rr + ((ln & 15) >> 1);
      p.out[(ln & 1) * 2048 + br * 1024 + col] = (unsigned short)(pack_bf16(z, z) & 0xFFFFu);
    }
    if (r == 0 && lane == 0) {
      const u64 nx = (((u64)tagv[1] << 32) | tagv[0]) + 1;
      const v2u e2 = {(unsigned)nx, (unsigned)(nx >> 32)};
      __builtin_amdgcn_raw_buffer_store_b64(e2, rws, WS_EPOCH + br * 256, 0, AUX_SC1);
    }
    STAMP(18);
    STAMP_FLUSH();
  }
}

}

extern "C" void kernel_launch(void* const* d_in, const int* in_sizes, int n_in, void* d_out, int out_size, void* d_ws,
                              size_t ws_size, hipStream_t stream) {
  (void)in_sizes; (void)n_in; (void)out_size; (void)ws_size;
  Params p;
  for (int i = 0; i < 2; ++i) {
    p.x[i] = (const unsigned short*)d_in[5 * i + 0];
    p.k[i] = (const unsigned short*)d_in[5 * i + 1];
    p.v[i] = (const unsigned short*)d_in[5 * i + 2];
    p.wq[i] = (const unsigned short*)d_in[5 * i + 3];
    p.wo[i] = (const unsigned short*)d_in[5 * i + 4];
  }
  p.out = (unsigned short*)d_out;
  p.ws = (unsigned char*)d_ws;
  hipLaunchKernelGGL(fused_decode, dim3(kNWG), dim3(kNThr), 0, stream, p);
}
```

```cpp
#include <hip/hip_runtime.h>
#include <hip/hip_bf16.h>
#include <stdint.h>

typedef unsigned v4u __attribute__((ext_vector_type(4)));
typedef unsigned v2u __attribute__((ext_vector_type(2)));
typedef float f32x4 __attribute__((ext_vector_type(4)));
typedef __bf16 bf16x8 __attribute__((ext_vector_type(8)));
typedef __bf16 bf16x2 __attribute__((ext_vector_type(2)));
typedef unsigned long long u64;
typedef __amdgpu_buffer_rsrc_t rsrc_t;

namespace {

constexpr int kD = 1024, kHkv = 4, kSk = 32768, kDh = 128;
constexpr int kNWG = 256, kNThr = 512;
constexpr int kUnitsS = 7;
#define SCHED_T0 400
#define SCHED_TAU 469
constexpr int AUX_NT = 2, AUX_SC1 = 16;
constexpr int RSRC_W3 = 0x00020000;
constexpr unsigned kSent = 0xFFFFFFFFu;
constexpr int kDrop = 0x7FFFFF00;
constexpr unsigned kSpinCap = 1u << 18;
constexpr float kC2 = 0.088388347648318440f * 1.4426950408889634f;

constexpr int WS_EPOCH = 0;
constexpr int WS_RDY = 1024;
constexpr int WS_TIN = 4096;
constexpr int WS_QG = 8192;
constexpr int kPartSlot = 4096;
constexpr int WS_PART = WS_QG + 8 * 2 * 2 * 1024 * 8;
constexpr int WS_OP = WS_PART + 256 * kPartSlot;
constexpr int WS_END = WS_OP + 256 * 8192;

constexpr int L_X = 0;
constexpr int L_TAG = L_X + 2048;
constexpr int L_CNT = L_TAG + 16;
constexpr int L_QRED = L_CNT + 32;
constexpr int L_QIMG = L_QRED + 4 * 512;
constexpr int L_OIMG = L_QIMG + 2048;
constexpr int L_MRGML = L_OIMG + 128;
constexpr int L_MRG = L_MRGML + 8 * 16 * 4;
constexpr int L_END = L_MRG + 8 * 8 * 132 * 4;

enum { C_QPART, C_QIMG, C_RESET, C_LAST, C_PART, C_TICKET, C_T0 };

struct Params {
  const unsigned short* x[2];
  const unsigned short* k[2];
  const unsigned short* v[2];
  const unsigned short* wq[2];
  const unsigned short* wo[2];
  unsigned short* out;
  unsigned char* ws;
};

__device__ __forceinline__ rsrc_t make_rsrc(const void* p, int bytes) {
  return __builtin_amdgcn_make_buffer_rsrc((void*)p, 0, bytes, RSRC_W3);
}
template <int AUX> __device__ __forceinline__ v4u ld16(rsrc_t r, int voff, int soff) {
  return __builtin_amdgcn_raw_buffer_load_b128(r, voff, soff, AUX);
}
__device__ __forceinline__ f32x4 mfma16(v4u a, v4u b, f32x4 c) {
  return __builtin_amdgcn_mfma_f32_16x16x32_bf16(__builtin_bit_cast(bf16x8, a), __builtin_bit_cast(bf16x8, b), c, 0, 0, 0);
}
__device__ __forceinline__ float dot2(unsigned a, unsigned b, float c) {
  return __builtin_amdgcn_fdot2_f32_bf16(__builtin_bit_cast(bf16x2, a), __builtin_bit_cast(bf16x2, b), c, false);
}
__device__ __forceinline__ unsigned pack_bf16(float lo, float hi) {
  bf16x2 r = {(__bf16)lo, (__bf16)hi};
  return __builtin_bit_cast(unsigned, r);
}
template <int CTRL> __device__ __forceinline__ float dpp(float x) {
  return __builtin_bit_cast(float, __builtin_amdgcn_mov_dpp(__builtin_bit_cast(int, x), CTRL, 0xf, 0xf, true));
}
constexpr int ROR2 = 0x122, ROR4 = 0x124, ROR8 = 0x128;
__device__ __forceinline__ float swap16_add(float a, float b) {
  auto s = __builtin_amdgcn_permlane16_swap(__float_as_uint(a), __float_as_uint(b), false, false);
  return __uint_as_float(s[0]) + __uint_as_float(s[1]);
}
__device__ __forceinline__ float swap32_add(float a, float b) {
  auto s = __builtin_amdgcn_permlane32_swap(__float_as_uint(a), __float_as_uint(b), false, false);
  return __uint_as_float(s[0]) + __uint_as_float(s[1]);
}
__device__ __forceinline__ float swap16_max(float a) {
  auto s = __builtin_amdgcn_permlane16_swap(__float_as_uint(a), __float_as_uint(a), false, false);
  return fmaxf(__uint_as_float(s[0]), __uint_as_float(s[1]));
}
__device__ __forceinline__ float swap32_max(float a) {
  auto s = __builtin_amdgcn_permlane32_swap(__float_as_uint(a), __float_as_uint(a), false, false);
  return fmaxf(__uint_as_float(s[0]), __uint_as_float(s[1]));
}
__device__ __forceinline__ unsigned perm_lo(unsigned hi, unsigned lo) { return __builtin_amdgcn_perm(hi, lo, 0x05040100u); }
__device__ __forceinline__ unsigned perm_hi(unsigned hi, unsigned lo) { return __builtin_amdgcn_perm(hi, lo, 0x07060302u); }
__device__ __forceinline__ v4u column_of(const v4u* r, int t) {
  v4u o;
#pragma unroll
  for (int u = 0; u < 4; ++u)
    o[u] = (t & 1) ? perm_hi(r[2 * u + 1][t >> 1], r[2 * u][t >> 1]) : perm_lo(r[2 * u + 1][t >> 1], r[2 * u][t >> 1]);
  return o;
}
__device__ __forceinline__ unsigned not_sentinel(float f) {
  const unsigned u = __float_as_uint(f);
  return u == kSent ? 0x7FC00000u : u;
}
__device__ __forceinline__ unsigned quiet(float f) { return f != f ? 0x7FC00000u : __float_as_uint(f); }
__device__ __forceinline__ unsigned umax4(v4u a) { return max(max(a[0], a[1]), max(a[2], a[3])); }
__device__ __forceinline__ u64 mix64(u64 z) {
  z += 0x9E3779B97F4A7C15ull;
  z = (z ^ (z >> 30)) * 0xBF58476D1CE4E5B9ull;
  z = (z ^ (z >> 27)) * 0x94D049BB133111EBull;
  return z ^ (z >> 31);
}
#define STAMP(i) do {} while (0)
#define STAMP_FLUSH() do {} while (0)
#define PIN() __builtin_amdgcn_sched_barrier(0)
#define MEMFENCE() asm volatile("" ::: "memory")
__device__ __forceinline__ void wg_barrier() {
  asm volatile("s_waitcnt lgkmcnt(0)" ::: "memory");
  __builtin_amdgcn_s_barrier();
  asm volatile("" ::: "memory");
}

__device__ __forceinline__ void lds_arrive(unsigned* c, int lane) {
  asm volatile("s_waitcnt lgkmcnt(0)" ::: "memory");
  if (lane == 0) __hip_atomic_fetch_add(c, 1u, __ATOMIC_RELAXED, __HIP_MEMORY_SCOPE_WORKGROUP);
}
template <int SLEEP = 0> __device__ __forceinline__ void lds_wait(unsigned* c, unsigned n) {
  while (__hip_atomic_load(c, __ATOMIC_RELAXED, __HIP_MEMORY_SCOPE_WORKGROUP) < n) __builtin_amdgcn_s_sleep(SLEEP);
  MEMFENCE();
}

__global__ void __launch_bounds__(kNThr, 2) fused_decode(Params p) {
  __shared__ __attribute__((aligned(16))) unsigned char lds[L_END];
  const int tid = threadIdx.x, lane = tid & 63;
  const int wave = __builtin_amdgcn_readfirstlane(tid >> 6);
  const bool streamer = wave < 4;
  const int wg = blockIdx.x, pair = wg >> 5, idx = wg & 31;
  const int br = pair >> 2, kvh = pair & 3, b = idx >> 4, split = idx & 15;
  const int qblk = idx & 15, kh = idx >> 4;
  const int hl = idx >> 2, rq = idx & 3;
  STAMP(0);
  const unsigned t_in = (unsigned)__builtin_amdgcn_s_memrealtime();
  PIN();
  const rsrc_t rws = make_rsrc(p.ws, WS_END);
  const size_t kv_off = ((size_t)(b * kHkv + kvh) * kSk + (size_t)(split * 64 + (streamer ? wave : 8 * kUnitsS - 4 + wave)) * 32) * kDh;
  const rsrc_t rk = make_rsrc(p.k[br] + kv_off, streamer ? (2 * kUnitsS - 1) * 32768 + 8192 : 32768 + 8192);
  const rsrc_t rv = make_rsrc(p.v[br] + kv_off, streamer ? (2 * kUnitsS - 1) * 32768 + 8192 : 32768 + 8192);

  unsigned* const cnt = reinterpret_cast<unsigned*>(lds + L_CNT);
  v4u wqr[16], xv;
  v2u ep;
  if (!streamer) {
    const int xw = wave - 4;
    const rsrc_t rq_ = make_rsrc(p.wq[br] + (size_t)(kh * 512 + xw * 128) * 4096 + kvh * 1024 + qblk * 64, 127 * 8192 + 128);
    const rsrc_t rx = make_rsrc(p.x[br] + kh * 512 + xw * 128, 2048 + 256);
    const int qo = (lane >> 3) * 16 * 8192 + (lane & 7) * 16;
    ep = __builtin_amdgcn_raw_buffer_load_b64(rws, wave == 7 ? WS_EPOCH + br * 256 : kDrop, 0, AUX_SC1);
    xv = ld16<0>(rx, lane < 32 ? (lane >> 4) * 2048 + (lane & 15) * 16 : kDrop, 0);
#pragma unroll
    for (int i = 0; i < 16; ++i) wqr[i] = ld16<AUX_NT>(rq_, qo, i * 8192);
    if (tid < 256 + 8) cnt[lane] = lane == C_T0 ? t_in : 0u;
  }
  PIN();
  wg_barrier();
  PIN();
  const int kl = (lane & 15) >> 1;
  int koff = (4 * (kl & 1) + (kl >> 1)) * 256 + (4 * (lane & 1) + (lane >> 4)) * 16;
  int voff = lane * 16;
  v4u Kt[2][8], Vt[2][8];
#define K_T0 SCHED_T0
#define K_TAU SCHED_TAU
#define K_GATE_WO 1
#define K_LAST 4u
#define GROUP_CAP() asm volatile("s_waitcnt vmcnt(4)" ::: "memory")
#define K_LIGHT 0
#define K_EARLY_RESET 1
#define K_TOUCH 1
#define NAP(n) do { for (int z_ = 0; z_ < (n); ++z_) __builtin_amdgcn_s_sleep(2); } while (0)
#define K_ANCHOR 1
#define K_D1 12
#define K_D2 20
#define K_DP 4
  unsigned t_gate = __builtin_amdgcn_readfirstlane(cnt[C_T0]) + K_T0;
#define GATE_(CAP)                                                                                                   \
  do {                                                                                                               \
    unsigned tk_ = 0;                                                                                                \
    if (lane == 0) tk_ = __hip_atomic_fetch_add(cnt + C_TICKET, 1u, __ATOMIC_RELAXED, __HIP_MEMORY_SCOPE_WORKGROUP); \
    const unsigned due_ = t_gate + ((__builtin_amdgcn_readfirstlane(tk_) * K_TAU) >> 4);                             \
    CAP;                                                                                                             \
    while ((int)((unsigned)__builtin_amdgcn_s_memrealtime() - due_) < 0) __builtin_amdgcn_s_sleep(1);               \
    MEMFENCE();                                                                                                      \
  } while (0)
#define GATE() GATE_(GROUP_CAP())
#define LOAD_K(buf, u)                                                                                               \
  do {                                                                                                               \
    GATE();                                                                                                          \
    asm volatile("" : "+v"(koff));                                                                                   \
    _Pragma("unroll") for (int i = 0; i < 8; ++i)                                                                    \
        Kt[buf][i] = ld16<AUX_NT>(rk, koff + (i & 2) * 1024 + (i & 1) * 128, (u) * 65536 + (buf) * 32768 + (i >> 2) * 4096); \
    PIN();                                                                                                           \
  } while (0)
#define LOAD_V(buf, u)                                                                                               \
  do {                                                                                                               \
    GATE();                                                                                                          \
    asm volatile("" : "+v"(voff));                                                                                   \
    _Pragma("unroll") for (int i = 0; i < 8; ++i)                                                                    \
        Vt[buf][i] = ld16<AUX_NT>(rv, voff + (i & 3) * 1024, (u) * 65536 + (buf) * 32768 + (i >> 2) * 4096);         \
    PIN();                                                                                                           \
  } while (0)
  if (streamer) {
    if (K_TOUCH) {
      const rsrc_t ro_ = make_rsrc(p.wo[br] + (size_t)((kvh * 8 + hl) * kDh + rq * 32) * kD, 32 * kD * 2);
      const int to = lane == 0 ? 0 : kDrop;
      unsigned t0_ = __builtin_amdgcn_raw_buffer_load_b32(rk, to, wave * 98304, AUX_NT);
      unsigned t1_ = __builtin_amdgcn_raw_buffer_load_b32(rv, to, wave * 98304, AUX_NT);
      unsigned t2_ = __builtin_amdgcn_raw_buffer_load_b32(ro_, to, wave * 16384, AUX_NT);
      asm volatile("" ::"v"(t0_), "v"(t1_), "v"(t2_));
    }
    if (K_EARLY_RESET) {
      const v4u s4 = {kSent, kSent, kSent, kSent};
      __builtin_amdgcn_raw_buffer_store_b128(s4, rws, WS_OP + wg * 8192 + tid * 16, 0, AUX_SC1);
      __builtin_amdgcn_raw_buffer_store_b128(s4, rws, WS_OP + wg * 8192 + 4096 + tid * 16, 0, AUX_SC1);
      __builtin_amdgcn_raw_buffer_store_b128(s4, rws, WS_PART + wg * kPartSlot + tid * 16, 0, AUX_SC1);
      asm volatile("s_waitcnt vmcnt(0)" ::: "memory");
      lds_arrive(cnt + C_RESET, lane);
    }
  } else {
    const int xw = wave - 4;
    if (lane < 32) *reinterpret_cast<v4u*>(lds + L_X + xw * 512 + lane * 16) = xv;
    asm volatile("s_waitcnt lgkmcnt(0)" ::: "memory");
    float qa[16];
#pragma unroll
    for (int i = 0; i < 16; ++i) qa[i] = 0.f;
#pragma unroll
    for (int h = 0; h < 2; ++h) {
      const v4u x0 = *reinterpret_cast<const v4u*>(lds + L_X + xw * 512 + (lane >> 3) * 32 + h * 16);
      const v4u x1 = *reinterpret_cast<const v4u*>(lds + L_X + xw * 512 + 256 + (lane >> 3) * 32 + h * 16);
#pragma unroll
      for (int e = 0; e < 8; ++e) {
        const v4u c = column_of(wqr + 8 * h, e);
#pragma unroll
        for (int u = 0; u < 4; ++u) {
          qa[e] = dot2(c[u], x0[u], qa[e]);
          qa[8 + e] = dot2(c[u], x1[u], qa[8 + e]);
        }
      }
    }
    float r8[8], r4[4];
#pragma unroll
    for (int e = 0; e < 8; ++e) r8[e] = swap32_add(qa[e], qa[8 + e]);
#pragma unroll
    for (int e = 0; e < 4; ++e) r4[e] = swap16_add(r8[e], r8[4 + e]);
    const bool up = lane & 8;
    const float o0 = (up ? r4[2] : r4[0]) + dpp<ROR8>(up ? r4[0] : r4[2]);
    const float o1 = (up ? r4[3] : r4[1]) + dpp<ROR8>(up ? r4[1] : r4[3]);
    *reinterpret_cast<float2*>(lds + L_QRED + xw * 512 + lane * 8) = make_float2(o0, o1);
    STAMP(1);
    lds_arrive(cnt + C_QPART, lane);
  }
  if (wave == 7) {
    const u64 epoch = ((u64)(unsigned)__builtin_amdgcn_readfirstlane(ep[1]) << 32) | (unsigned)__builtin_amdgcn_readfirstlane(ep[0]);
    const u64 tg = mix64(epoch);
    const unsigned t8 = (unsigned)tg & 0xFF000000u, th = (unsigned)(tg >> 32);
    if (lane == 0) *reinterpret_cast<v4u*>(lds + L_TAG) = v4u{(unsigned)epoch, (unsigned)(epoch >> 32), (unsigned)tg, th};
    lds_wait(cnt + C_QPART, 4u);
    float s0 = 0.f, s1 = 0.f;
#pragma unroll
    for (int w = 0; w < 4; ++w) {
      const float2 q = *reinterpret_cast<const float2*>(lds + L_QRED + w * 512 + lane * 8);
      s0 += q.x;
      s1 += q.y;
    }
    const v4u g = {t8 | (__float_as_uint(s0) >> 8), th, t8 | (__float_as_uint(s1) >> 8), th};
    const int col = 8 * (lane & 7) + 4 * ((lane >> 4) & 1) + 2 * ((lane >> 3) & 1);
    __builtin_amdgcn_raw_buffer_store_b128(g, rws, WS_QG + (((pair * 2 + (lane >> 5)) * 2 + kh) * 1024 + qblk * 64 + col) * 8, 0, AUX_SC1);
    const unsigned t_own = t_gate - K_T0;
    __builtin_amdgcn_raw_buffer_store_b64(v2u{t8 | (t_own & 0xFFFFFFu), th}, rws, lane == 0 ? WS_TIN + wg * 8 : kDrop, 0, AUX_SC1);
    v2u tin;
    STAMP(2);
    const int qg_off = WS_QG + (pair * 2 + b) * 16384 + lane * 16;
    v4u qg[16];
    unsigned spins = 0;
    unsigned bad;
    if (K_LIGHT) {
      const int lo = lane < 32 ? WS_QG + (pair * 2 + b) * 16384 + lane * 512 : kDrop;
      do {
        MEMFENCE();
        const v2u g1 = __builtin_amdgcn_raw_buffer_load_b64(rws, lo, 0, AUX_SC1);
        bad = lane < 32 ? ((g1[0] ^ t8) >> 24) | (g1[1] ^ th) : 0u;
      } while (__any(bad != 0u) && ++spins < kSpinCap);
      STAMP(23);
    }
    do {
      MEMFENCE();
#pragma unroll
      for (int i = 0; i < 16; ++i) qg[i] = ld16<AUX_SC1>(rws, qg_off, i * 1024);
      tin = __builtin_amdgcn_raw_buffer_load_b64(rws, WS_TIN + pair * 256 + (lane & 31) * 8, 0, AUX_SC1);
      unsigned bl = tin[0] ^ t8, bh = tin[1] ^ th;
#pragma unroll
      for (int i = 0; i < 16; ++i) {
        bl |= (qg[i][0] ^ t8) | (qg[i][2] ^ t8);
        bh |= (qg[i][1] ^ th) | (qg[i][3] ^ th);
      }
      bad = (bl >> 24) | bh;
    } while (__any(bad != 0u) && ++spins < kSpinCap);
#pragma unroll
    for (int i = 0; i < 8; ++i)
      *reinterpret_cast<unsigned*>(lds + L_QIMG + i * 256 + lane * 4) =
          pack_bf16(__uint_as_float(qg[i][0] << 8) + __uint_as_float(qg[8 + i][0] << 8), __uint_as_float(qg[i][2] << 8) + __uint_as_float(qg[8 + i][2] << 8));
    {
      int lead = (int)((t_own - tin[0]) << 8) >> 8;
      lead = max(lead, __builtin_amdgcn_mov_dpp(lead, 0xB1, 0xf, 0xf, true));
      lead = max(lead, __builtin_amdgcn_mov_dpp(lead, 0x4E, 0xf, 0xf, true));
      lead = max(lead, __builtin_amdgcn_mov_dpp(lead, ROR4, 0xf, 0xf, true));
      lead = max(lead, __builtin_amdgcn_mov_dpp(lead, ROR8, 0xf, 0xf, true));
      lead = max(__builtin_amdgcn_readlane(lead, 0), __builtin_amdgcn_readlane(lead, 16));
      if (lane == 0) cnt[C_T0] = t_own - (unsigned)(K_ANCHOR ? min(lead, 1000) : 0);
    }
    STAMP(3);
    lds_arrive(cnt + C_QIMG, lane);
  }
  PIN();
  LOAD_K(0, 0);
  LOAD_V(0, 0);
  LOAD_K(1, 0);
  LOAD_V(1, 0);
  if (wave != 7) STAMP(3);
  lds_wait<1>(cnt + C_QIMG, 1u);
  t_gate = __builtin_amdgcn_readfirstlane(cnt[C_T0]) + K_T0;
  const v4u tagv = *reinterpret_cast<const v4u*>(lds + L_TAG);
  const unsigned tag_lo = __builtin_amdgcn_readfirstlane(tagv[2]), tag_hi = __builtin_amdgcn_readfirstlane(tagv[3]);
  STAMP(4);
  const int pp = (lane >> 3) & 1, hh = lane & 7, gg = lane >> 4;
  const v4u Q0 = *reinterpret_cast<const v4u*>(lds + L_QIMG + hh * 256 + pp * 64 + gg * 16);
  const v4u Q1 = *reinterpret_cast<const v4u*>(lds + L_QIMG + hh * 256 + 128 + pp * 64 + gg * 16);

  f32x4 acc[8];
#pragma unroll
  for (int t = 0; t < 8; ++t) acc[t] = f32x4{0.f, 0.f, 0.f, 0.f};
  float m = -INFINITY, l = 0.f;
  const unsigned pmask = pp ? 0xFFFF0000u : 0x0000FFFFu;
#define TILE(buf, AFTER_QK, AFTER_PV)                                                                                \
  do {                                                                                                               \
    float s[4];                                                                                                      \
    _Pragma("unroll") for (int T = 0; T < 4; ++T) {                                                                  \
      f32x4 d = mfma16(Kt[buf][2 * T], Q0, f32x4{0.f, 0.f, 0.f, 0.f});                                               \
      d = mfma16(Kt[buf][2 * T + 1], Q1, d);                                                                         \
      s[T] = (pp ? d[3] : d[0]) + dpp<ROR8>(pp ? d[1] : d[2]);                                                       \
    }                                                                                                                \
    PIN();                                                                                                           \
    AFTER_QK;                                                                                                        \
    float mt = fmaxf(fmaxf(s[0], s[1]), fmaxf(s[2], s[3]));                                                          \
    mt = fmaxf(mt, dpp<ROR8>(mt));                                                                                   \
    mt = swap32_max(swap16_max(mt)) * kC2;                                                                           \
    const float mn = fmaxf(m, mt);                                                                                   \
    if (__any(mn != m)) {                                                                                            \
      const float alpha = __builtin_amdgcn_exp2f(m - mn);                                                            \
      l *= alpha;                                                                                                    \
      _Pragma("unroll") for (int i = 0; i < 8; ++i) acc[i] *= alpha;                                                 \
      m = mn;                                                                                                        \
    }                                                                                                                \
    v4u P;                                                                                                           \
    _Pragma("unroll") for (int T = 0; T < 4; ++T) {                                                                  \
      const float e = __builtin_amdgcn_exp2f(fmaf(s[T], kC2, -m));                                                   \
      l += e;                                                                                                        \
      P[T] = pack_bf16(e, e) & pmask;                                                                                \
    }                                                                                                                \
    _Pragma("unroll") for (int i = 0; i < 8; ++i) acc[i] = mfma16(column_of(Vt[buf], i), P, acc[i]);                 \
    PIN();                                                                                                           \
    AFTER_PV;                                                                                                        \
  } while (0)
  const int nunit = streamer ? kUnitsS : 1;
#pragma unroll 1
  for (int u = 1; u < nunit; ++u) {
    TILE(0, LOAD_K(0, u), LOAD_V(0, u));
    TILE(1, LOAD_K(1, u), LOAD_V(1, u));
  }
  v4u rdy = ld16<AUX_SC1>(rws, streamer ? lane * 16 : kDrop, WS_RDY + br * 1024);
  PIN();
  STAMP(12);
  if (streamer) lds_arrive(cnt + C_LAST, lane);
  PIN();
  TILE(0, PIN(), PIN());
  TILE(1, PIN(), PIN());
#undef TILE
#undef LOAD_K
#undef LOAD_V
  STAMP(13);

  l += dpp<ROR8>(l);
  l = swap16_add(l, l);
  l = swap32_add(l, l);
  {
    float* dst = reinterpret_cast<float*>(lds + L_MRG) + (wave * 8 + hh) * 132 + 32 * gg + 16 * pp;
#pragma unroll
    for (int rr = 0; rr < 2; ++rr)
#pragma unroll
      for (int q = 0; q < 2; ++q) {
        f32x4 o;
#pragma unroll
        for (int e = 0; e < 4; ++e) {
          const f32x4 a = acc[4 * q + e];
          o[e] = (pp ? a[2 + rr] : a[rr]) + dpp<ROR8>(pp ? a[rr] : a[2 + rr]);
        }
        *reinterpret_cast<f32x4*>(dst + 8 * rr + 4 * q) = o;
      }
    if (lane < 8) {
      float* ml = reinterpret_cast<float*>(lds + L_MRGML) + lane * 16;
      ml[wave] = m;
      ml[8 + wave] = l;
    }
  }
  lds_arrive(cnt + C_PART, lane);
  if (streamer) {
    STAMP(7);
    lds_wait(cnt + C_PART, 8u);
    lds_wait(cnt + C_RESET, 4u);
    STAMP(8);
    {
      const int hd = 2 * wave + (lane >> 5);
      const float* ml = reinterpret_cast<const float*>(lds + L_MRGML) + hd * 16;
      const f32x4 m0 = *reinterpret_cast<const f32x4*>(ml), m1 = *reinterpret_cast<const f32x4*>(ml + 4);
      const f32x4 l0 = *reinterpret_cast<const f32x4*>(ml + 8), l1 = *reinterpret_cast<const f32x4*>(ml + 12);
      f32x4 o[8];
#pragma unroll
      for (int w = 0; w < 8; ++w) o[w] = *reinterpret_cast<const f32x4*>(lds + L_MRG + ((w * 8 + hd) * 132 + 4 * (lane & 31)) * 4);
      const float M = fmaxf(fmaxf(fmaxf(m0[0], m0[1]), fmaxf(m0[2], m0[3])), fmaxf(fmaxf(m1[0], m1[1]), fmaxf(m1[2], m1[3])));
      float L = 0.f;
      f32x4 os = {0.f, 0.f, 0.f, 0.f};
#pragma unroll
      for (int w = 0; w < 8; ++w) {
        const float e = __builtin_amdgcn_exp2f((w < 4 ? m0[w & 3] : m1[w & 3]) - M);
        L = fmaf(e, w < 4 ? l0[w & 3] : l1[w & 3], L);
        os += e * o[w];
      }
      const float inv = __builtin_amdgcn_rcpf(L);
      const unsigned u0 = quiet(os[0] * inv), u1 = quiet(os[1] * inv), u2 = quiet(os[2] * inv), u3 = quiet(os[3] * inv);
      const v4u ov = {quiet(M + __builtin_amdgcn_logf(L)), __builtin_amdgcn_perm(u0, u1, 0x07060501u), __builtin_amdgcn_perm(u2, u1, 0x07060302u),
                      __builtin_amdgcn_perm(u3, u2, 0x07060501u)};
      __builtin_amdgcn_raw_buffer_store_b128(ov, rws, WS_PART + wg * kPartSlot + wave * 1024 + lane * 16, 0, AUX_SC1);
    }
    PIN();
    STAMP(14);
#define RDY_BAD() __any(((rdy[0] ^ tag_lo) | (rdy[1] ^ tag_hi) | (rdy[2] ^ tag_lo) | (rdy[3] ^ tag_hi)) != 0u)
    if (RDY_BAD()) {
      unsigned spins = 0;
      do {
        __builtin_amdgcn_s_sleep(2);
        MEMFENCE();
        rdy = ld16<AUX_SC1>(rws, lane * 16, WS_RDY + br * 1024);
      } while (RDY_BAD() && ++spins < kSpinCap);
    }
#undef RDY_BAD
    MEMFENCE();
    const int po = WS_PART + (pair * 32 + (lane >> 1)) * kPartSlot + idx * 128 + 32 * wave + 16 * (lane & 1);
    NAP(K_D1);
    MEMFENCE();
    v4u pd = ld16<AUX_SC1>(rws, po, 0);
    for (unsigned spins = 0;; ++spins) {
      const bool ok = umax4(pd) != kSent;
      if (__all(ok) || spins > kSpinCap) break;
      NAP(K_DP);
      MEMFENCE();
      if (!ok) pd = ld16<AUX_SC1>(rws, po, 0);
    }
    STAMP(15);
    const float ms = __uint_as_float(pd[0]);
    float M = fmaxf(ms, dpp<ROR2>(ms));
    M = fmaxf(M, dpp<ROR4>(M));
    M = fmaxf(M, dpp<ROR8>(M));
    M = swap16_max(M);
    const float e = __builtin_amdgcn_exp2f(ms - M);
    float v[5] = {e, e * __uint_as_float(pd[1] & 0xFFFFFF00u), e * __uint_as_float(__builtin_amdgcn_perm(pd[2], pd[1], 0x0504000Cu)),
                  e * __uint_as_float(__builtin_amdgcn_perm(pd[2], pd[3], 0x0706000Cu)), e * __uint_as_float(pd[3] & 0xFFFFFF00u)};
#pragma unroll
    for (int i = 0; i < 5; ++i) v[i] += dpp<ROR2>(v[i]);
#pragma unroll
    for (int i = 0; i < 5; ++i) v[i] += dpp<ROR4>(v[i]);
#pragma unroll
    for (int i = 0; i < 5; ++i) v[i] += dpp<ROR8>(v[i]);
#pragma unroll
    for (int i = 0; i < 5; ++i) v[i] = swap16_add(v[i], v[i]);
    const float inv = __builtin_amdgcn_rcpf(v[0]);
    const v2u ob = {pack_bf16(v[1] * inv, v[2] * inv), pack_bf16(v[3] * inv, v[4] * inv)};
    if ((lane & 30) == 0) *reinterpret_cast<v2u*>(lds + L_OIMG + (lane >> 5) * 64 + wave * 16 + (lane & 1) * 8) = ob;
    STAMP(10);
    wg_barrier();
    if (wave != 0) { STAMP_FLUSH(); return; }
  } else {
    {
      const v4u s4 = {kSent, kSent, kSent, kSent};
      const int xt = tid - 256;
      if (!K_EARLY_RESET) {
        __builtin_amdgcn_raw_buffer_store_b128(s4, rws, WS_OP + wg * 8192 + xt * 16, 0, AUX_SC1);
        __builtin_amdgcn_raw_buffer_store_b128(s4, rws, WS_OP + wg * 8192 + 4096 + xt * 16, 0, AUX_SC1);
        __builtin_amdgcn_raw_buffer_store_b128(s4, rws, WS_PART + wg * kPartSlot + xt * 16, 0, AUX_SC1);
        asm volatile("s_waitcnt vmcnt(0)" ::: "memory");
        lds_arrive(cnt + C_RESET, lane);
      }
      if (wave == 4) {
        lds_wait<1>(cnt + C_RESET, 4u);
        const v2u t2 = {tag_lo, tag_hi};
        __builtin_amdgcn_raw_buffer_store_b64(t2, rws, lane == 0 ? WS_RDY + wg * 8 : kDrop, 0, AUX_SC1);
      }
    }
    lds_wait<4>(cnt + C_LAST, K_LAST);
    PIN();
    v4u wor[16], wc[16];
    const rsrc_t ro = make_rsrc(p.wo[br] + (size_t)((kvh * 8 + hl) * kDh + rq * 32) * kD, 32 * kD * 2);
#pragma unroll
    for (int i = 0; i < 16; ++i) {
      if ((i & 7) == 0 && K_GATE_WO) GATE_(MEMFENCE());
      wor[i] = ld16<AUX_NT>(ro, gg * 8 * 2048 + (lane & 15) * 16, (i & 7) * 2048 + (i >> 3) * 256 + (wave & 3) * 512);
    }
    PIN();
    STAMP(7);
#pragma unroll
    for (int e = 0; e < 16; ++e) {
      wc[e] = column_of(wor + (e & 8), e & 7);
      asm volatile("" : "+v"(wc[e]));
    }
    STAMP(10);
    wg_barrier();
    STAMP(11);
    const v4u of = *reinterpret_cast<const v4u*>(lds + L_OIMG + (lane & 1) * 64 + gg * 16);
    const int le = (lane & 15) >> 1;
    f32x4 d0 = {0.f, 0.f, 0.f, 0.f}, d1 = {0.f, 0.f, 0.f, 0.f};
#pragma unroll
    for (int e = 0; e < 8; ++e) {
      const v4u z = {0u, 0u, 0u, 0u};
      const v4u bf = le == e ? of : z;
      d0 = mfma16(wc[e], bf, d0);
      d1 = mfma16(wc[8 + e], bf, d1);
    }
    const v4u o0 = {not_sentinel(d0[0]), not_sentinel(d0[1]), not_sentinel(d0[2]), not_sentinel(d0[3])};
    const v4u o1 = {not_sentinel(d1[0]), not_sentinel(d1[1]), not_sentinel(d1[2]), not_sentinel(d1[3])};
    const int so = WS_OP + wg * 8192 + (wave & 3) * 2048 + lane * 16;
    __builtin_amdgcn_raw_buffer_store_b128(o0, rws, so, 0, AUX_SC1);
    __builtin_amdgcn_raw_buffer_store_b128(o1, rws, so + 1024, 0, AUX_SC1);
    STAMP(16);
    STAMP_FLUSH();
    return;
  }

  {
    const int r = wg & 127;
    const int fo = WS_OP + (br * 128 + (lane >> 2)) * 8192 + r * 64 + (lane & 3) * 16;
    v4u f[8];
    NAP(K_D2);
    MEMFENCE();
#pragma unroll
    for (int q = 0; q < 8; ++q) f[q] = ld16<AUX_SC1>(rws, fo, q * 16 * 8192);
    for (unsigned spins = 0;; ++spins) {
      unsigned mx = 0u;
#pragma unroll
      for (int q = 0; q < 8; ++q) mx = max(mx, umax4(f[q]));
      const bool ok = mx != kSent;
      if (__all(ok) || spins > kSpinCap) break;
      NAP(K_DP);
      MEMFENCE();
      if (!ok) {
#pragma unroll
        for (int q = 0; q < 8; ++q) f[q] = ld16<AUX_SC1>(rws, fo, q * 16 * 8192);
      }
    }
    STAMP(17);
    float y[4];
#pragma unroll
    for (int e = 0; e < 4; ++e) {
      y[e] = ((__uint_as_float(f[0][e]) + __uint_as_float(f[1][e])) + (__uint_as_float(f[2][e]) + __uint_as_float(f[3][e]))) +
             ((__uint_as_float(f[4][e]) + __uint_as_float(f[5][e])) + (__uint_as_float(f[6][e]) + __uint_as_float(f[7][e])));
    }
    const float z0 = swap32_add(y[0], y[2]), z1 = swap32_add(y[1], y[3]);
    float z = swap16_add(z0, z1);
    z += dpp<ROR4>(z);
    z += dpp<ROR8>(z);
    if ((lane & 12) == 0) {
      const int pos = 4 * r + (lane & 3), ln = pos & 63, rr = lane >> 4;
      const int col = 128 * (pos >> 6) + 32 * (ln >> 4) + 8 * rr + ((ln & 15) >> 1);
      p.out[(ln & 1) * 2048 + br * 1024 + col] = (unsigned short)(pack_bf16(z, z) & 0xFFFFu);
    }
    if (r == 0 && lane == 0) {
      const u64 nx = (((u64)tagv[1] << 32) | tagv[0]) + 1;
      const v2u e2 = {(unsigned)nx, (unsigned)(nx >> 32)};
      __builtin_amdgcn_raw_buffer_store_b64(e2, rws, WS_EPOCH + br * 256, 0, AUX_SC1);
    }
    STAMP(18);
    STAMP_FLUSH();
  }
}

}

extern "C" void kernel_launch(void* const* d_in, const int* in_sizes, int n_in, void* d_out, int out_size, void* d_ws,
                              size_t ws_size, hipStream_t stream) {
  (void)in_sizes; (void)n_in; (void)out_size; (void)ws_size;
  Params p;
  for (int i = 0; i < 2; ++i) {
    p.x[i] = (const unsigned short*)d_in[5 * i + 0];
    p.k[i] = (const unsigned short*)d_in[5 * i + 1];
    p.v[i] = (const unsigned short*)d_in[5 * i + 2];
    p.wq[i] = (const unsigned short*)d_in[5 * i + 3];
    p.wo[i] = (const unsigned short*)d_in[5 * i + 4];
  }
  p.out = (unsigned short*)d_out;
  p.ws = (unsigned char*)d_ws;
  hipLaunchKernelGGL(fused_decode, dim3(kNWG), dim3(kNThr), 0, stream, p);
}
```

```cpp
#include <hip/hip_runtime.h>
#include <hip/hip_bf16.h>
#include <stdint.h>

typedef unsigned v4u __attribute__((ext_vector_type(4)));
typedef unsigned v2u __attribute__((ext_vector_type(2)));
typedef float f32x4 __attribute__((ext_vector_type(4)));
typedef __bf16 bf16x8 __attribute__((ext_vector_type(8)));
typedef __bf16 bf16x2 __attribute__((ext_vector_type(2)));
typedef unsigned long long u64;
typedef __amdgpu_buffer_rsrc_t rsrc_t;

namespace {

constexpr int kD = 1024, kHkv = 4, kSk = 32768, kDh = 128;
constexpr int kNWG = 256, kNThr = 512;
constexpr int kUnitsS = 7;
#define SCHED_T0 400
#define SCHED_TAU 476
constexpr int AUX_NT = 2, AUX_SC1 = 16;
constexpr int RSRC_W3 = 0x00020000;
constexpr unsigned kSent = 0xFFFFFFFFu;
constexpr int kDrop = 0x7FFFFF00;
constexpr unsigned kSpinCap = 1u << 18;
constexpr float kC2 = 0.088388347648318440f * 1.4426950408889634f;

constexpr int WS_EPOCH = 0;
constexpr int WS_RDY = 2048;
constexpr int WS_TIN = 4096;
constexpr int WS_QG = 8192;
constexpr int kPartSlot = 4096;
constexpr int WS_PART = WS_QG + 8 * 2 * 2 * 1024 * 8;
constexpr int WS_OP = WS_PART + 256 * kPartSlot;
constexpr int WS_END = WS_OP + 256 * 8192;

constexpr int L_X = 0;
constexpr int L_TAG = L_X + 2048;
constexpr int L_CNT = L_TAG + 16;
constexpr int L_QRED = L_CNT + 32;
constexpr int L_QIMG = L_QRED + 4 * 512;
constexpr int L_OIMG = L_QIMG + 2048;
constexpr int L_MRGML = L_OIMG + 128;
constexpr int L_MRG = L_MRGML + 8 * 16 * 4;
constexpr int L_END = L_MRG + 8 * 8 * 132 * 4;

enum { C_QPART, C_QIMG, C_RESET, C_LAST, C_PART, C_TICKET, C_T0 };

struct Params {
  const unsigned short* x[2];
  const unsigned short* k[2];
  const unsigned short* v[2];
  const unsigned short* wq[2];
  const unsigned short* wo[2];
  unsigned short* out;
  unsigned char* ws;
};

__device__ __forceinline__ rsrc_t make_rsrc(const void* p, int bytes) {
  return __builtin_amdgcn_make_buffer_rsrc((void*)p, 0, bytes, RSRC_W3);
}
template <int AUX> __device__ __forceinline__ v4u ld16(rsrc_t r, int voff, int soff) {
  return __builtin_amdgcn_raw_buffer_load_b128(r, voff, soff, AUX);
}
__device__ __forceinline__ f32x4 mfma16(v4u a, v4u b, f32x4 c) {
  return __builtin_amdgcn_mfma_f32_16x16x32_bf16(__builtin_bit_cast(bf16x8, a), __builtin_bit_cast(bf16x8, b), c, 0, 0, 0);
}
__device__ __forceinline__ float dot2(unsigned a, unsigned b, float c) {
  return __builtin_amdgcn_fdot2_f32_bf16(__builtin_bit_cast(bf16x2, a), __builtin_bit_cast(bf16x2, b), c, false);
}
__device__ __forceinline__ unsigned pack_bf16(float lo, float hi) {
  bf16x2 r = {(__bf16)lo, (__bf16)hi};
  return __builtin_bit_cast(unsigned, r);
}
template <int CTRL> __device__ __forceinline__ float dpp(float x) {
  return __builtin_bit_cast(float, __builtin_amdgcn_mov_dpp(__builtin_bit_cast(int, x), CTRL, 0xf, 0xf, true));
}
constexpr int ROR2 = 0x122, ROR4 = 0x124, ROR8 = 0x128;
__device__ __forceinline__ float swap16_add(float a, float b) {
  auto s = __builtin_amdgcn_permlane16_swap(__float_as_uint(a), __float_as_uint(b), false, false);
  return __uint_as_float(s[0]) + __uint_as_float(s[1]);
}
__device__ __forceinline__ float swap32_add(float a, float b) {
  auto s = __builtin_amdgcn_permlane32_swap(__float_as_uint(a), __float_as_uint(b), false, false);
  return __uint_as_float(s[0]) + __uint_as_float(s[1]);
}
__device__ __forceinline__ float swap16_max(float a) {
  auto s = __builtin_amdgcn_permlane16_swap(__float_as_uint(a), __float_as_uint(a), false, false);
  return fmaxf(__uint_as_float(s[0]), __uint_as_float(s[1]));
}
__device__ __forceinline__ float swap32_max(float a) {
  auto s = __builtin_amdgcn_permlane32_swap(__float_as_uint(a), __float_as_uint(a), false, false);
  return fmaxf(__uint_as_float(s[0]), __uint_as_float(s[1]));
}
__device__ __forceinline__ unsigned perm_lo(unsigned hi, unsigned lo) { return __builtin_amdgcn_perm(hi, lo, 0x05040100u); }
__device__ __forceinline__ unsigned perm_hi(unsigned hi, unsigned lo) { return __builtin_amdgcn_perm(hi, lo, 0x07060302u); }
__device__ __forceinline__ v4u column_of(const v4u* r, int t) {
  v4u o;
#pragma unroll
  for (int u = 0; u < 4; ++u)
    o[u] = (t & 1) ? perm_hi(r[2 * u + 1][t >> 1], r[2 * u][t >> 1]) : perm_lo(r[2 * u + 1][t >> 1], r[2 * u][t >> 1]);
  return o;
}
__device__ __forceinline__ unsigned not_sentinel(float f) { return min(__float_as_uint(f), kSent - 1u); }
__device__ __forceinline__ unsigned quiet(float f) { return f != f ? 0x7FC00000u : __float_as_uint(f); }
__device__ __forceinline__ unsigned umax4(v4u a) { return max(max(a[0], a[1]), max(a[2], a[3])); }
__device__ __forceinline__ u64 mix64(u64 z) {
  z += 0x9E3779B97F4A7C15ull;
  z = (z ^ (z >> 30)) * 0xBF58476D1CE4E5B9ull;
  z = (z ^ (z >> 27)) * 0x94D049BB133111EBull;
  return z ^ (z >> 31);
}
#define STAMP(i) do {} while (0)
#define STAMP_FLUSH() do {} while (0)
#define PIN() __builtin_amdgcn_sched_barrier(0)
#define MEMFENCE() asm volatile("" ::: "memory")
__device__ __forceinline__ void wg_barrier() {
  asm volatile("s_waitcnt lgkmcnt(0)" ::: "memory");
  __builtin_amdgcn_s_barrier();
  asm volatile("" ::: "memory");
}

__device__ __forceinline__ void lds_arrive(unsigned* c, int lane) {
  asm volatile("s_waitcnt lgkmcnt(0)" ::: "memory");
  if (lane == 0) __hip_atomic_fetch_add(c, 1u, __ATOMIC_RELAXED, __HIP_MEMORY_SCOPE_WORKGROUP);
}
template <int SLEEP = 0> __device__ __forceinline__ void lds_wait(unsigned* c, unsigned n) {
  while (__hip_atomic_load(c, __ATOMIC_RELAXED, __HIP_MEMORY_SCOPE_WORKGROUP) < n) __builtin_amdgcn_s_sleep(SLEEP);
  MEMFENCE();
}

__global__ void __launch_bounds__(kNThr, 2) fused_decode(Params p) {
  __shared__ __attribute__((aligned(16))) unsigned char lds[L_END];
  const int tid = threadIdx.x, lane = tid & 63;
  const int wave = __builtin_amdgcn_readfirstlane(tid >> 6);
  const bool streamer = wave < 4;
  const int wg = blockIdx.x, pair = wg >> 5, idx = wg & 31;
  const int br = pair >> 2, kvh = pair & 3, b = idx >> 4, split = idx & 15;
  const int qblk = idx & 15, kh = idx >> 4;
  const int hl = idx >> 2, rq = idx & 3;
  STAMP(0);
  const unsigned t_in = (unsigned)__builtin_amdgcn_s_memrealtime();
  PIN();
  const rsrc_t rws = make_rsrc(p.ws, WS_END);
  const int rot = 0;
  const size_t kv_off = ((size_t)(b * kHkv + kvh) * kSk + (size_t)(split * 64 + (streamer ? 0 : 56)) * 32) * kDh;
  const rsrc_t rk = make_rsrc(p.k[br] + kv_off, streamer ? kUnitsS * 65536 : 65536);
  const rsrc_t rv = make_rsrc(p.v[br] + kv_off, streamer ? kUnitsS * 65536 : 65536);
  const int toff0 = (((wave & 3) + rot) & 7) * 8192, toff1 = toff0 ^ 32768;

  unsigned* const cnt = reinterpret_cast<unsigned*>(lds + L_CNT);
  v4u wqr[16], xv;
  v2u ep;
  if (!streamer) {
    const int xw = wave - 4;
    const rsrc_t rq_ = make_rsrc(p.wq[br] + (size_t)(kh * 512 + xw * 128) * 4096 + kvh * 1024 + qblk * 64, 127 * 8192 + 128);
    const rsrc_t rx = make_rsrc(p.x[br] + kh * 512 + xw * 128, 2048 + 256);
    const int qo = (lane >> 3) * 16 * 8192 + (lane & 7) * 16;
    asm volatile("" :: "s"(rq_), "s"(rx));
    STAMP(28);
    ep = __builtin_amdgcn_raw_buffer_load_b64(rws, wave == 7 ? WS_EPOCH + br * 256 : kDrop, 0, AUX_SC1);
    xv = ld16<0>(rx, lane < 32 ? (lane >> 4) * 2048 + (lane & 15) * 16 : kDrop, 0);
#pragma unroll
    for (int i = 0; i < 16; ++i) wqr[i] = ld16<AUX_NT>(rq_, qo, i * 8192);
    STAMP(29);
    if (tid < 256 + 8) cnt[lane] = lane == C_T0 ? t_in : 0u;
  }
  PIN();
  wg_barrier();
  PIN();
  const int kl = (lane & 15) >> 1;
  int koff = (4 * (kl & 1) + (kl >> 1)) * 256 + (4 * (lane & 1) + (lane >> 4)) * 16;
  int voff = lane * 16;
  v4u Kt[2][8], Vt[2][8];
#define K_T0 SCHED_T0
#define K_TAU SCHED_TAU
#define K_TAU_WO SCHED_TAU
#define K_GATE_WO 1
#define K_LAST 3u
#define GROUP_CAP() asm volatile("s_waitcnt vmcnt(4)" ::: "memory")
#define K_LIGHT 0
#define K_GAP 0
#define K_QT 540
#define K_PHASE 1
#define K_EARLY_RESET 1
#define K_TOUCH 1
#define NAP(n) do { for (int z_ = 0; z_ < (n); ++z_) __builtin_amdgcn_s_sleep(2); } while (0)
#define K_ANCHOR 1
#define K_D1 12
#define K_D2 20
#define K_DP 4
  unsigned t_gate = __builtin_amdgcn_readfirstlane(cnt[C_T0]) + K_T0;
#define GATE_(CAP)                                                                                                   \
  do {                                                                                                               \
    unsigned tk_ = 0;                                                                                                \
    if (lane == 0) tk_ = __hip_atomic_fetch_add(cnt + C_TICKET, 1u, __ATOMIC_RELAXED, __HIP_MEMORY_SCOPE_WORKGROUP); \
    tk_ = __builtin_amdgcn_readfirstlane(tk_);                                                                       \
    const unsigned due_ = t_gate + ((min(tk_, 128u) * K_TAU + (max(tk_, 128u) - 128u) * K_TAU_WO) >> 4);             \
    CAP;                                                                                                             \
    while ((int)((unsigned)__builtin_amdgcn_s_memrealtime() - due_) < 0) __builtin_amdgcn_s_sleep(1);               \
    MEMFENCE();                                                                                                      \
  } while (0)
#define GATE() GATE_(GROUP_CAP())
#define LOAD_K(buf, u)                                                                                               \
  do {                                                                                                               \
    GATE();                                                                                                          \
    asm volatile("" : "+v"(koff));                                                                                   \
    _Pragma("unroll") for (int i = 0; i < 8; ++i)                                                                    \
        Kt[buf][i] = ld16<AUX_NT>(rk, koff + (i & 2) * 1024 + (i & 1) * 128, (u) * 65536 + ((buf) ? toff1 : toff0) + (i >> 2) * 4096); \
    PIN();                                                                                                           \
  } while (0)
#define LOAD_V(buf, u)                                                                                               \
  do {                                                                                                               \
    GATE();                                                                                                          \
    asm volatile("" : "+v"(voff));                                                                                   \
    _Pragma("unroll") for (int i = 0; i < 8; ++i)                                                                    \
        Vt[buf][i] = ld16<AUX_NT>(rv, voff + (i & 3) * 1024, (u) * 65536 + ((buf) ? toff1 : toff0) + (i >> 2) * 4096); \
    PIN();                                                                                                           \
  } while (0)
  if (streamer) {
    if (K_TOUCH) {
      const rsrc_t ro_ = make_rsrc(p.wo[br] + (size_t)((kvh * 8 + hl) * kDh + rq * 32) * kD, 32 * kD * 2);
      const int to = lane == 0 ? 0 : kDrop;
      unsigned t0_ = __builtin_amdgcn_raw_buffer_load_b32(rk, to, wave * 98304, AUX_NT);
      unsigned t1_ = __builtin_amdgcn_raw_buffer_load_b32(rv, to, wave * 98304, AUX_NT);
      unsigned t2_ = __builtin_amdgcn_raw_buffer_load_b32(ro_, to, wave * 16384, AUX_NT);
      asm volatile("" ::"v"(t0_), "v"(t1_), "v"(t2_));
    }
    {
      const v4u s4 = {kSent, kSent, kSent, kSent};
      __builtin_amdgcn_raw_buffer_store_b128(s4, rws, WS_OP + wg * 8192 + tid * 16, 0, AUX_SC1);
      __builtin_amdgcn_raw_buffer_store_b128(s4, rws, WS_OP + wg * 8192 + 4096 + tid * 16, 0, AUX_SC1);
      __builtin_amdgcn_raw_buffer_store_b128(s4, rws, WS_PART + wg * kPartSlot + tid * 16, 0, AUX_SC1);
      asm volatile("s_waitcnt vmcnt(0)" ::: "memory");
      lds_arrive(cnt + C_RESET, lane);
    }
  } else {
    const int xw = wave - 4;
    if (lane < 32) *reinterpret_cast<v4u*>(lds + L_X + xw * 512 + lane * 16) = xv;
    asm volatile("s_waitcnt lgkmcnt(0)" ::: "memory");
    float qa[16];
#pragma unroll
    for (int i = 0; i < 16; ++i) qa[i] = 0.f;
#pragma unroll
    for (int h = 0; h < 2; ++h) {
      const v4u x0 = *reinterpret_cast<const v4u*>(lds + L_X + xw * 512 + (lane >> 3) * 32 + h * 16);
      const v4u x1 = *reinterpret_cast<const v4u*>(lds + L_X + xw * 512 + 256 + (lane >> 3) * 32 + h * 16);
#pragma unroll
      for (int e = 0; e < 8; ++e) {
        const v4u c = column_of(wqr + 8 * h, e);
#pragma unroll
        for (int u = 0; u < 4; ++u) {
          qa[e] = dot2(c[u], x0[u], qa[e]);
          qa[8 + e] = dot2(c[u], x1[u], qa[8 + e]);
        }
      }
    }
    float r8[8], r4[4];
#pragma unroll
    for (int e = 0; e < 8; ++e) r8[e] = swap32_add(qa[e], qa[8 + e]);
#pragma unroll
    for (int e = 0; e < 4; ++e) r4[e] = swap16_add(r8[e], r8[4 + e]);
    const bool up = lane & 8;
    const float o0 = (up ? r4[2] : r4[0]) + dpp<ROR8>(up ? r4[0] : r4[2]);
    const float o1 = (up ? r4[3] : r4[1]) + dpp<ROR8>(up ? r4[1] : r4[3]);
    *reinterpret_cast<float2*>(lds + L_QRED + xw * 512 + lane * 8) = make_float2(o0, o1);
    STAMP(1);
    lds_arrive(cnt + C_QPART, lane);
  }
  if (wave == 7) {
    const u64 epoch = ((u64)(unsigned)__builtin_amdgcn_readfirstlane(ep[1]) << 32) | (unsigned)__builtin_amdgcn_readfirstlane(ep[0]);
    const u64 tg = mix64(epoch);
    const unsigned t8 = (unsigned)tg & 0xFF000000u, th = (unsigned)(tg >> 32);
    if (lane == 0) *reinterpret_cast<v4u*>(lds + L_TAG) = v4u{(unsigned)epoch, (unsigned)(epoch >> 32), (unsigned)tg, th};
    lds_wait(cnt + C_QPART, 4u);
    float s0 = 0.f, s1 = 0.f;
#pragma unroll
    for (int w = 0; w < 4; ++w) {
      const float2 q = *reinterpret_cast<const float2*>(lds + L_QRED + w * 512 + lane * 8);
      s0 += q.x;
      s1 += q.y;
    }
    const v4u g = {t8 | (__float_as_uint(s0) >> 8), th, t8 | (__float_as_uint(s1) >> 8), th};
    const int col = 8 * (lane & 7) + 4 * ((lane >> 4) & 1) + 2 * ((lane >> 3) & 1);
    __builtin_amdgcn_raw_buffer_store_b128(g, rws, WS_QG + (((pair * 2 + (lane >> 5)) * 2 + kh) * 1024 + qblk * 64 + col) * 8, 0, AUX_SC1);
    const unsigned t_own = t_gate - K_T0;
    __builtin_amdgcn_raw_buffer_store_b64(v2u{t8 | (t_own & 0xFFFFFFu), th}, rws, lane == 0 ? WS_TIN + wg * 8 : kDrop, 0, AUX_SC1);
    lds_wait(cnt + C_RESET, 4u);
    __builtin_amdgcn_raw_buffer_store_b64(v2u{t8, th}, rws, lane == 0 ? WS_RDY + wg * 8 : kDrop, 0, AUX_SC1);
    v2u tin;
    STAMP(2);
    while ((int)((unsigned)__builtin_amdgcn_s_memrealtime() - (t_own + K_QT)) < 0) __builtin_amdgcn_s_sleep(1);
    const int qg_off = WS_QG + (pair * 2 + b) * 16384 + lane * 16;
    v4u qg[16];
    unsigned spins = 0;
    unsigned bad;
    if (K_LIGHT) {
      const int lo = lane < 32 ? WS_QG + (pair * 2 + b) * 16384 + lane * 512 : kDrop;
      do {
        MEMFENCE();
        const v2u g1 = __builtin_amdgcn_raw_buffer_load_b64(rws, lo, 0, AUX_SC1);
        bad = lane < 32 ? ((g1[0] ^ t8) >> 24) | (g1[1] ^ th) : 0u;
      } while (__any(bad != 0u) && ++spins < kSpinCap);
      STAMP(23);
    }
    do {
      MEMFENCE();
#pragma unroll
      for (int i = 0; i < 16; ++i) qg[i] = ld16<AUX_SC1>(rws, qg_off, i * 1024);
      tin = __builtin_amdgcn_raw_buffer_load_b64(rws, WS_TIN + pair * 256 + (lane & 31) * 8, 0, AUX_SC1);
      unsigned bl = tin[0] ^ t8, bh = tin[1] ^ th;
#pragma unroll
      for (int i = 0; i < 16; ++i) {
        bl |= (qg[i][0] ^ t8) | (qg[i][2] ^ t8);
        bh |= (qg[i][1] ^ th) | (qg[i][3] ^ th);
      }
      bad = (bl >> 24) | bh;
    } while (__any(bad != 0u) && ++spins < kSpinCap);
#pragma unroll
    for (int i = 0; i < 8; ++i)
      *reinterpret_cast<unsigned*>(lds + L_QIMG + i * 256 + lane * 4) =
          pack_bf16(__uint_as_float(qg[i][0] << 8) + __uint_as_float(qg[8 + i][0] << 8), __uint_as_float(qg[i][2] << 8) + __uint_as_float(qg[8 + i][2] << 8));
    {
      int lead = (int)((t_own - tin[0]) << 8) >> 8;
      lead = max(lead, __builtin_amdgcn_mov_dpp(lead, 0xB1, 0xf, 0xf, true));
      lead = max(lead, __builtin_amdgcn_mov_dpp(lead, 0x4E, 0xf, 0xf, true));
      lead = max(lead, __builtin_amdgcn_mov_dpp(lead, ROR4, 0xf, 0xf, true));
      lead = max(lead, __builtin_amdgcn_mov_dpp(lead, ROR8, 0xf, 0xf, true));
      lead = max(__builtin_amdgcn_readlane(lead, 0), __builtin_amdgcn_readlane(lead, 16));
      if (lane == 0) cnt[C_T0] = t_own - (unsigned)(K_ANCHOR ? min(lead, 1000) : 0) - (K_PHASE ? (unsigned)((wg * 37) & 31) : 0u);
    }
    STAMP(3);
    lds_arrive(cnt + C_QIMG, lane);
  }
  PIN();
  LOAD_K(0, 0);
  LOAD_V(0, 0);
  LOAD_K(1, 0);
  LOAD_V(1, 0);
  if (wave != 7) STAMP(3);
  lds_wait<1>(cnt + C_QIMG, 1u);
  t_gate = __builtin_amdgcn_readfirstlane(cnt[C_T0]) + K_T0;
  const v4u tagv = *reinterpret_cast<const v4u*>(lds + L_TAG);
  const unsigned tag_lo = __builtin_amdgcn_readfirstlane(tagv[2]), tag_hi = __builtin_amdgcn_readfirstlane(tagv[3]);
  STAMP(4);
  const int pp = (lane >> 3) & 1, hh = lane & 7, gg = lane >> 4;
  const v4u Q0 = *reinterpret_cast<const v4u*>(lds + L_QIMG + hh * 256 + pp * 64 + gg * 16);
  const v4u Q1 = *reinterpret_cast<const v4u*>(lds + L_QIMG + hh * 256 + 128 + pp * 64 + gg * 16);

  f32x4 acc[8];
#pragma unroll
  for (int t = 0; t < 8; ++t) acc[t] = f32x4{0.f, 0.f, 0.f, 0.f};
  float m = -INFINITY, l = 0.f;
  const unsigned pmask = pp ? 0xFFFF0000u : 0x0000FFFFu;
#define TILE(buf, AFTER_QK, AFTER_PV)                                                                                \
  do {                                                                                                               \
    float s[4];                                                                                                      \
    _Pragma("unroll") for (int T = 0; T < 4; ++T) {                                                                  \
      f32x4 d = mfma16(Kt[buf][2 * T], Q0, f32x4{0.f, 0.f, 0.f, 0.f});                                               \
      d = mfma16(Kt[buf][2 * T + 1], Q1, d);                                                                         \
      s[T] = (pp ? d[3] : d[0]) + dpp<ROR8>(pp ? d[1] : d[2]);                                                       \
    }                                                                                                                \
    PIN();                                                                                                           \
    AFTER_QK;                                                                                                        \
    float mt = fmaxf(fmaxf(s[0], s[1]), fmaxf(s[2], s[3]));                                                          \
    mt = fmaxf(mt, dpp<ROR8>(mt));                                                                                   \
    mt = swap32_max(swap16_max(mt)) * kC2;                                                                           \
    const float mn = fmaxf(m, mt);                                                                                   \
    if (__any(mn != m)) {                                                                                            \
      const float alpha = __builtin_amdgcn_exp2f(m - mn);                                                            \
      l *= alpha;                                                                                                    \
      _Pragma("unroll") for (int i = 0; i < 8; ++i) acc[i] *= alpha;                                                 \
      m = mn;                                                                                                        \
    }                                                                                                                \
    v4u P;                                                                                                           \
    _Pragma("unroll") for (int T = 0; T < 4; ++T) {                                                                  \
      const float e = __builtin_amdgcn_exp2f(fmaf(s[T], kC2, -m));                                                   \
      l += e;                                                                                                        \
      P[T] = pack_bf16(e, e) & pmask;                                                                                \
    }                                                                                                                \
    _Pragma("unroll") for (int i = 0; i < 8; ++i) acc[i] = mfma16(column_of(Vt[buf], i), P, acc[i]);                 \
    PIN();                                                                                                           \
    AFTER_PV;                                                                                                        \
  } while (0)
  const int nunit = streamer ? kUnitsS : 1;
#pragma unroll 1
  for (int u = 1; u < nunit; ++u) {
    TILE(0, LOAD_K(0, u), LOAD_V(0, u));
    TILE(1, LOAD_K(1, u), LOAD_V(1, u));
  }
  v4u rdy = ld16<AUX_SC1>(rws, streamer ? lane * 16 : kDrop, WS_RDY + br * 1024);
  PIN();
  STAMP(12);
  if (streamer) lds_arrive(cnt + C_LAST, lane);
  PIN();
  TILE(0, PIN(), PIN());
  TILE(1, PIN(), PIN());
#undef TILE
#undef LOAD_K
#undef LOAD_V
  STAMP(13);

  l += dpp<ROR8>(l);
  l = swap16_add(l, l);
  l = swap32_add(l, l);
  {
    float* dst = reinterpret_cast<float*>(lds + L_MRG) + (wave * 8 + hh) * 132 + 32 * gg + 16 * pp;
#pragma unroll
    for (int rr = 0; rr < 2; ++rr)
#pragma unroll
      for (int q = 0; q < 2; ++q) {
        f32x4 o;
#pragma unroll
        for (int e = 0; e < 4; ++e) {
          const f32x4 a = acc[4 * q + e];
          o[e] = (pp ? a[2 + rr] : a[rr]) + dpp<ROR8>(pp ? a[rr] : a[2 + rr]);
        }
        *reinterpret_cast<f32x4*>(dst + 8 * rr + 4 * q) = o;
      }
    if (lane < 8) {
      float* ml = reinterpret_cast<float*>(lds + L_MRGML) + lane * 16;
      ml[wave] = m;
      ml[8 + wave] = l;
    }
  }
  lds_arrive(cnt + C_PART, lane);
  if (streamer) {
    STAMP(7);
    lds_wait(cnt + C_PART, 8u);
    STAMP(8);
    {
      const int hd = 2 * wave + (lane >> 5);
      const float* ml = reinterpret_cast<const float*>(lds + L_MRGML) + hd * 16;
      const f32x4 m0 = *reinterpret_cast<const f32x4*>(ml), m1 = *reinterpret_cast<const f32x4*>(ml + 4);
      const f32x4 l0 = *reinterpret_cast<const f32x4*>(ml + 8), l1 = *reinterpret_cast<const f32x4*>(ml + 12);
      f32x4 o[8];
#pragma unroll
      for (int w = 0; w < 8; ++w) o[w] = *reinterpret_cast<const f32x4*>(lds + L_MRG + ((w * 8 + hd) * 132 + 4 * (lane & 31)) * 4);
      const float M = fmaxf(fmaxf(fmaxf(m0[0], m0[1]), fmaxf(m0[2], m0[3])), fmaxf(fmaxf(m1[0], m1[1]), fmaxf(m1[2], m1[3])));
      float L = 0.f;
      f32x4 os = {0.f, 0.f, 0.f, 0.f};
#pragma unroll
      for (int w = 0; w < 8; ++w) {
        const float e = __builtin_amdgcn_exp2f((w < 4 ? m0[w & 3] : m1[w & 3]) - M);
        L = fmaf(e, w < 4 ? l0[w & 3] : l1[w & 3], L);
        os += e * o[w];
      }
      const float inv = __builtin_amdgcn_rcpf(L);
      const unsigned u0 = quiet(os[0] * inv), u1 = quiet(os[1] * inv), u2 = quiet(os[2] * inv), u3 = quiet(os[3] * inv);
      const v4u ov = {quiet(M + __builtin_amdgcn_logf(L)), __builtin_amdgcn_perm(u0, u1, 0x07060501u), __builtin_amdgcn_perm(u2, u1, 0x07060302u),
                      __builtin_amdgcn_perm(u3, u2, 0x07060501u)};
      __builtin_amdgcn_raw_buffer_store_b128(ov, rws, WS_PART + wg * kPartSlot + wave * 1024 + lane * 16, 0, AUX_SC1);
    }
    PIN();
    STAMP(14);
#define RDY_BAD() __any(((((rdy[0] ^ tag_lo) | (rdy[2] ^ tag_lo)) >> 24) | (rdy[1] ^ tag_hi) | (rdy[3] ^ tag_hi)) != 0u)
    if (RDY_BAD()) {
      unsigned spins = 0;
      do {
        __builtin_amdgcn_s_sleep(2);
        MEMFENCE();
        rdy = ld16<AUX_SC1>(rws, lane * 16, WS_RDY + br * 1024);
      } while (RDY_BAD() && ++spins < kSpinCap);
    }
#undef RDY_BAD
    MEMFENCE();
    const int po = WS_PART + (pair * 32 + (lane >> 1)) * kPartSlot + idx * 128 + 32 * wave + 16 * (lane & 1);
    NAP(K_D1);
    MEMFENCE();
    v4u pd = ld16<AUX_SC1>(rws, po, 0);
    for (unsigned spins = 0;; ++spins) {
      const bool ok = umax4(pd) != kSent;
      if (__all(ok) || spins > kSpinCap) break;
      NAP(K_DP);
      MEMFENCE();
      if (!ok) pd = ld16<AUX_SC1>(rws, po, 0);
    }
    STAMP(15);
    const float ms = __uint_as_float(pd[0]);
    float M = fmaxf(ms, dpp<ROR2>(ms));
    M = fmaxf(M, dpp<ROR4>(M));
    M = fmaxf(M, dpp<ROR8>(M));
    M = swap16_max(M);
    const float e = __builtin_amdgcn_exp2f(ms - M);
    float v[5] = {e, e * __uint_as_float(pd[1] & 0xFFFFFF00u), e * __uint_as_float(__builtin_amdgcn_perm(pd[2], pd[1], 0x0504000Cu)),
                  e * __uint_as_float(__builtin_amdgcn_perm(pd[2], pd[3], 0x0706000Cu)), e * __uint_as_float(pd[3] & 0xFFFFFF00u)};
#pragma unroll
    for (int i = 0; i < 5; ++i) v[i] += dpp<ROR2>(v[i]);
#pragma unroll
    for (int i = 0; i < 5; ++i) v[i] += dpp<ROR4>(v[i]);
#pragma unroll
    for (int i = 0; i < 5; ++i) v[i] += dpp<ROR8>(v[i]);
#pragma unroll
    for (int i = 0; i < 5; ++i) v[i] = swap16_add(v[i], v[i]);
    const float inv = __builtin_amdgcn_rcpf(v[0]);
    const v2u ob = {pack_bf16(v[1] * inv, v[2] * inv), pack_bf16(v[3] * inv, v[4] * inv)};
    if ((lane & 30) == 0) *reinterpret_cast<v2u*>(lds + L_OIMG + (lane >> 5) * 64 + wave * 16 + (lane & 1) * 8) = ob;
    STAMP(10);
    wg_barrier();
    if (wave != 0) { STAMP_FLUSH(); return; }
  } else {
    lds_wait<4>(cnt + C_LAST, K_LAST);
    PIN();
    v4u wor[16], wc[16];
    const rsrc_t ro = make_rsrc(p.wo[br] + (size_t)((kvh * 8 + hl) * kDh + rq * 32) * kD, 32 * kD * 2);
#pragma unroll
    for (int i = 0; i < 16; ++i) {
      if ((i & 7) == 0 && K_GATE_WO) GATE_(MEMFENCE());
      wor[i] = ld16<AUX_NT>(ro, gg * 8 * 2048 + (lane & 15) * 16, (i & 7) * 2048 + (i >> 3) * 256 + (wave & 3) * 512);
    }
    PIN();
    STAMP(7);
#pragma unroll
    for (int e = 0; e < 16; ++e) {
      wc[e] = column_of(wor + (e & 8), e & 7);
      asm volatile("" : "+v"(wc[e]));
    }
    STAMP(10);
    wg_barrier();
    STAMP(11);
    const v4u of = *reinterpret_cast<const v4u*>(lds + L_OIMG + (lane & 1) * 64 + gg * 16);
    const int le = (lane & 15) >> 1;
    f32x4 d0 = {0.f, 0.f, 0.f, 0.f}, d1 = {0.f, 0.f, 0.f, 0.f};
#pragma unroll
    for (int e = 0; e < 8; ++e) {
      const v4u z = {0u, 0u, 0u, 0u};
      const v4u bf = le == e ? of : z;
      d0 = mfma16(wc[e], bf, d0);
      d1 = mfma16(wc[8 + e], bf, d1);
    }
    const v4u o0 = {not_sentinel(d0[0]), not_sentinel(d0[1]), not_sentinel(d0[2]), not_sentinel(d0[3])};
    const v4u o1 = {not_sentinel(d1[0]), not_sentinel(d1[1]), not_sentinel(d1[2]), not_sentinel(d1[3])};
    const int so = WS_OP + wg * 8192 + (wave & 3) * 2048 + lane * 16;
    __builtin_amdgcn_raw_buffer_store_b128(o0, rws, so, 0, AUX_SC1);
    __builtin_amdgcn_raw_buffer_store_b128(o1, rws, so + 1024, 0, AUX_SC1);
    STAMP(16);
    STAMP_FLUSH();
    return;
  }

  {
    const int r = wg & 127;
    const int fo = WS_OP + (br * 128 + (lane >> 2)) * 8192 + r * 64 + (lane & 3) * 16;
    v4u f[8];
    NAP(K_D2);
    MEMFENCE();
#pragma unroll
    for (int q = 0; q < 8; ++q) f[q] = ld16<AUX_SC1>(rws, fo, q * 16 * 8192);
    for (unsigned spins = 0;; ++spins) {
      unsigned mx = 0u;
#pragma unroll
      for (int q = 0; q < 8; ++q) mx = max(mx, umax4(f[q]));
      const bool ok = mx != kSent;
      if (__all(ok) || spins > kSpinCap) break;
      NAP(K_DP);
      MEMFENCE();
      if (!ok) {
#pragma unroll
        for (int q = 0; q < 8; ++q) f[q] = ld16<AUX_SC1>(rws, fo, q * 16 * 8192);
      }
    }
    STAMP(17);
    float y[4];
#pragma unroll
    for (int e = 0; e < 4; ++e) {
      y[e] = ((__uint_as_float(f[0][e]) + __uint_as_float(f[1][e])) + (__uint_as_float(f[2][e]) + __uint_as_float(f[3][e]))) +
             ((__uint_as_float(f[4][e]) + __uint_as_float(f[5][e])) + (__uint_as_float(f[6][e]) + __uint_as_float(f[7][e])));
    }
    const float z0 = swap32_add(y[0], y[2]), z1 = swap32_add(y[1], y[3]);
    float z = swap16_add(z0, z1);
    z += dpp<ROR4>(z);
    z += dpp<ROR8>(z);
    if ((lane & 12) == 0) {
      const int pos = 4 * r + (lane & 3), ln = pos & 63, rr = lane >> 4;
      const int col = 128 * (pos >> 6) + 32 * (ln >> 4) + 8 * rr + ((ln & 15) >> 1);
      p.out[(ln & 1) * 2048 + br * 1024 + col] = (unsigned short)(pack_bf16(z, z) & 0xFFFFu);
    }
    if (r == 0 && lane == 0) {
      const u64 nx = (((u64)tagv[1] << 32) | tagv[0]) + 1;
      const v2u e2 = {(unsigned)nx, (unsigned)(nx >> 32)};
      __builtin_amdgcn_raw_buffer_store_b64(e2, rws, WS_EPOCH + br * 256, 0, AUX_SC1);
    }
    STAMP(18);
    STAMP_FLUSH();
  }
}

}

extern "C" void kernel_launch(void* const* d_in, const int* in_sizes, int n_in, void* d_out, int out_size, void* d_ws,
                              size_t ws_size, hipStream_t stream) {
  (void)in_sizes; (void)n_in; (void)out_size; (void)ws_size;
  Params p;
  for (int i = 0; i < 2; ++i) {
    p.x[i] = (const unsigned short*)d_in[5 * i + 0];
    p.k[i] = (const unsigned short*)d_in[5 * i + 1];
    p.v[i] = (const unsigned short*)d_in[5 * i + 2];
    p.wq[i] = (const unsigned short*)d_in[5 * i + 3];
    p.wo[i] = (const unsigned short*)d_in[5 * i + 4];
  }
  p.out = (unsigned short*)d_out;
  p.ws = (unsigned char*)d_ws;
  hipLaunchKernelGGL(fused_decode, dim3(kNWG), dim3(kNThr), 0, stream, p);
}
```

```cpp
#include <hip/hip_runtime.h>
#include <hip/hip_bf16.h>
#include <stdint.h>

typedef unsigned v4u __attribute__((ext_vector_type(4)));
typedef unsigned v2u __attribute__((ext_vector_type(2)));
typedef float f32x4 __attribute__((ext_vector_type(4)));
typedef __bf16 bf16x8 __attribute__((ext_vector_type(8)));
typedef __bf16 bf16x2 __attribute__((ext_vector_type(2)));
typedef unsigned long long u64;
typedef __amdgpu_buffer_rsrc_t rsrc_t;

namespace {

constexpr int kD = 1024, kHkv = 4, kSk = 32768, kDh = 128;
constexpr int kNWG = 256, kNThr = 512;
constexpr int kUnitsS = 7;
constexpr int AUX_NT = 2, AUX_SC1 = 16;
constexpr int RSRC_W3 = 0x00020000;
constexpr unsigned kSent = 0xFFFFFFFFu;
constexpr int kDrop = 0x7FFFFF00;
constexpr unsigned kSpinCap = 1u << 18;
constexpr float kC2 = 0.088388347648318440f * 1.4426950408889634f;

#define SCHED_T0 400
#define SCHED_TAU 465
constexpr unsigned kT0 = SCHED_T0;
constexpr unsigned kTau16 = SCHED_TAU;
constexpr unsigned kTGather = 540;
constexpr int kNap1 = 12, kNap2 = 20, kNapPoll = 4;

constexpr int WS_EPOCH = 0;
constexpr int WS_RDY = 2048;
constexpr int WS_TIN = 4096;
constexpr int WS_QG = 8192;
constexpr int kPartSlot = 4096;
constexpr int WS_PART = WS_QG + 8 * 2 * 2 * 1024 * 8;
constexpr int WS_OP = WS_PART + 256 * kPartSlot;
constexpr int WS_END = WS_OP + 256 * 8192;

constexpr int L_X = 0;
constexpr int L_TAG = L_X + 2048;
constexpr int L_CNT = L_TAG + 16;
constexpr int L_QRED = L_CNT + 32;
constexpr int L_QIMG = L_QRED + 4 * 512;
constexpr int L_OIMG = L_QIMG + 2048;
constexpr int L_ZERO = L_OIMG + 128;
constexpr int L_MRGML = L_ZERO + 16;
constexpr int L_MRG = L_MRGML + 8 * 16 * 4;
constexpr int L_END = L_MRG + 8 * 8 * 132 * 4;

enum { C_QPART, C_QIMG, C_RESET, C_LAST, C_PART, C_TICKET, C_T0 };

struct Params {
  const unsigned short* x[2];
  const unsigned short* k[2];
  const unsigned short* v[2];
  const unsigned short* wq[2];
  const unsigned short* wo[2];
  unsigned short* out;
  unsigned char* ws;
};

__device__ __forceinline__ rsrc_t make_rsrc(const void* p, int bytes) {
  return __builtin_amdgcn_make_buffer_rsrc((void*)p, 0, bytes, RSRC_W3);
}
template <int AUX> __device__ __forceinline__ v4u ld16(rsrc_t r, int voff, int soff) {
  return __builtin_amdgcn_raw_buffer_load_b128(r, voff, soff, AUX);
}
__device__ __forceinline__ f32x4 mfma16(v4u a, v4u b, f32x4 c) {
  return __builtin_amdgcn_mfma_f32_16x16x32_bf16(__builtin_bit_cast(bf16x8, a), __builtin_bit_cast(bf16x8, b), c, 0, 0, 0);
}
__device__ __forceinline__ float dot2(unsigned a, unsigned b, float c) {
  return __builtin_amdgcn_fdot2_f32_bf16(__builtin_bit_cast(bf16x2, a), __builtin_bit_cast(bf16x2, b), c, false);
}
__device__ __forceinline__ unsigned pack_bf16(float lo, float hi) {
  bf16x2 r = {(__bf16)lo, (__bf16)hi};
  return __builtin_bit_cast(unsigned, r);
}
template <int CTRL> __device__ __forceinline__ float dpp(float x) {
  return __builtin_bit_cast(float, __builtin_amdgcn_mov_dpp(__builtin_bit_cast(int, x), CTRL, 0xf, 0xf, true));
}
constexpr int XOR1 = 0xB1, XOR2 = 0x4E, ROR4 = 0x124, ROR8 = 0x128;
__device__ __forceinline__ float swap16_add(float a, float b) {
  auto s = __builtin_amdgcn_permlane16_swap(__float_as_uint(a), __float_as_uint(b), false, false);
  return __uint_as_float(s[0]) + __uint_as_float(s[1]);
}
__device__ __forceinline__ float swap32_add(float a, float b) {
  auto s = __builtin_amdgcn_permlane32_swap(__float_as_uint(a), __float_as_uint(b), false, false);
  return __uint_as_float(s[0]) + __uint_as_float(s[1]);
}
__device__ __forceinline__ float swap16_max(float a) {
  auto s = __builtin_amdgcn_permlane16_swap(__float_as_uint(a), __float_as_uint(a), false, false);
  return fmaxf(__uint_as_float(s[0]), __uint_as_float(s[1]));
}
__device__ __forceinline__ float swap32_max(float a) {
  auto s = __builtin_amdgcn_permlane32_swap(__float_as_uint(a), __float_as_uint(a), false, false);
  return fmaxf(__uint_as_float(s[0]), __uint_as_float(s[1]));
}
__device__ __forceinline__ v4u column_of(const v4u* r, int t) {
  v4u o;
#pragma unroll
  for (int u = 0; u < 4; ++u)
    o[u] = __builtin_amdgcn_perm(r[2 * u + 1][t >> 1], r[2 * u][t >> 1], (t & 1) ? 0x07060302u : 0x05040100u);
  return o;
}
__device__ __forceinline__ unsigned not_sentinel(float f) { return min(__float_as_uint(f), kSent - 1u); }
__device__ __forceinline__ unsigned quiet(float f) { return f != f ? 0x7FC00000u : __float_as_uint(f); }
__device__ __forceinline__ unsigned umax4(v4u a) { return max(max(a[0], a[1]), max(a[2], a[3])); }
__device__ __forceinline__ u64 mix64(u64 z) {
  z += 0x9E3779B97F4A7C15ull;
  z = (z ^ (z >> 30)) * 0xBF58476D1CE4E5B9ull;
  z = (z ^ (z >> 27)) * 0x94D049BB133111EBull;
  return z ^ (z >> 31);
}
#define PIN() __builtin_amdgcn_sched_barrier(0)
#define MEMFENCE() asm volatile("" ::: "memory")
__device__ __forceinline__ void wg_barrier() {
  asm volatile("s_waitcnt lgkmcnt(0)" ::: "memory");
  __builtin_amdgcn_s_barrier();
  MEMFENCE();
}
__device__ __forceinline__ void lds_arrive(unsigned* c, int lane) {
  asm volatile("s_waitcnt lgkmcnt(0)" ::: "memory");
  if (lane == 0) __hip_atomic_fetch_add(c, 1u, __ATOMIC_RELAXED, __HIP_MEMORY_SCOPE_WORKGROUP);
}
template <int SLEEP = 0> __device__ __forceinline__ void lds_wait(unsigned* c, unsigned n) {
  while (__hip_atomic_load(c, __ATOMIC_RELAXED, __HIP_MEMORY_SCOPE_WORKGROUP) < n) __builtin_amdgcn_s_sleep(SLEEP);
  MEMFENCE();
}
__device__ __forceinline__ void wait_until(unsigned t) {
  while ((int)((unsigned)__builtin_amdgcn_s_memrealtime() - t) < 0) __builtin_amdgcn_s_sleep(1);
  MEMFENCE();
}
__device__ __forceinline__ void nap(int n) {
  for (int i = 0; i < n; ++i) __builtin_amdgcn_s_sleep(2);
  MEMFENCE();
}

__global__ void __launch_bounds__(kNThr, 2) fused_decode(Params p) {
  __shared__ __attribute__((aligned(16))) unsigned char lds[L_END];
  const int tid = threadIdx.x, lane = tid & 63;
  const int wave = __builtin_amdgcn_readfirstlane(tid >> 6);
  const bool streamer = wave < 4;
  const int wg = blockIdx.x, pair = wg >> 5, idx = wg & 31;
  const int br = pair >> 2, kvh = pair & 3, b = idx >> 4, split = idx & 15;
  const int qblk = idx & 15, kh = idx >> 4;
  const unsigned t_in = (unsigned)__builtin_amdgcn_s_memrealtime();
  PIN();
  const rsrc_t rws = make_rsrc(p.ws, WS_END);
  const rsrc_t ro = make_rsrc(p.wo[br] + (size_t)(kvh * 1024 + idx * 32) * kD, 32 * kD * 2);
  const size_t kv_off = ((size_t)(b * kHkv + kvh) * kSk + (size_t)split * 2048) * kDh;
  const rsrc_t rk = make_rsrc(p.k[br] + kv_off, 2048 * kDh * 2);
  const rsrc_t rv = make_rsrc(p.v[br] + kv_off, 2048 * kDh * 2);
  const int toff0 = ((wave + wg + (wg >> 3) - (streamer ? 0 : 4)) & 7) * 8192, toff1 = toff0 ^ 32768;
  const int blk0 = (wg >> 3) + (wg >> 6) * 3 + (streamer ? 0 : 7);

  unsigned* const cnt = reinterpret_cast<unsigned*>(lds + L_CNT);
  v4u wqr[16], xv;
  v2u ep;
  if (!streamer) {
    const int xw = wave - 4;
    const rsrc_t rq = make_rsrc(p.wq[br] + (size_t)(kh * 512 + xw * 128) * 4096 + kvh * 1024 + qblk * 64, 127 * 8192 + 128);
    const rsrc_t rx = make_rsrc(p.x[br] + kh * 512 + xw * 128, 2048 + 256);
    const int qo = (lane >> 3) * 16 * 8192 + (lane & 7) * 16;
    ep = __builtin_amdgcn_raw_buffer_load_b64(rws, wave == 7 ? WS_EPOCH + br * 256 : kDrop, 0, AUX_SC1);
    xv = ld16<0>(rx, lane < 32 ? (lane >> 4) * 2048 + (lane & 15) * 16 : kDrop, 0);
#pragma unroll
    for (int i = 0; i < 16; ++i) wqr[i] = ld16<AUX_NT>(rq, qo, i * 8192);
    if (tid < 256 + 8) cnt[lane] = lane == C_T0 ? t_in : 0u;
    if (tid == 320) *reinterpret_cast<v4u*>(lds + L_ZERO) = v4u{0u, 0u, 0u, 0u};
  }
  PIN();
  wg_barrier();
  PIN();
  const int kl = (lane & 15) >> 1;
  int koff = (4 * (kl & 1) + (kl >> 1)) * 256 + (4 * (lane & 1) + (lane >> 4)) * 16;
  int voff = lane * 16;
  v4u Kt[2][8], Vt[2][8];
  unsigned t_gate = __builtin_amdgcn_readfirstlane(cnt[C_T0]) + kT0;
#define GATE(CAP)                                                                                                    \
  do {                                                                                                               \
    unsigned tk_ = 0;                                                                                                \
    if (lane == 0) tk_ = __hip_atomic_fetch_add(cnt + C_TICKET, 1u, __ATOMIC_RELAXED, __HIP_MEMORY_SCOPE_WORKGROUP); \
    const unsigned due_ = t_gate + ((__builtin_amdgcn_readfirstlane(tk_) * kTau16) >> 4);                            \
    if (CAP) asm volatile("s_waitcnt vmcnt(4)" ::: "memory");                                                        \
    wait_until(due_);                                                                                                \
  } while (0)
#define LOAD_K(buf, u)                                                                                               \
  do {                                                                                                               \
    GATE(true);                                                                                                      \
    asm volatile("" : "+v"(koff));                                                                                   \
    const int so_ = ((blk0 + (u)) & 7) * 65536 + ((buf) ? toff1 : toff0);                                            \
    _Pragma("unroll") for (int i = 0; i < 8; ++i)                                                                    \
        Kt[buf][i] = ld16<AUX_NT>(rk, koff + (i & 2) * 1024 + (i & 1) * 128, so_ + (i >> 2) * 4096);                 \
    PIN();                                                                                                           \
  } while (0)
#define LOAD_V(buf, u)                                                                                               \
  do {                                                                                                               \
    GATE(true);                                                                                                      \
    asm volatile("" : "+v"(voff));                                                                                   \
    const int so_ = ((blk0 + (u)) & 7) * 65536 + ((buf) ? toff1 : toff0);                                            \
    _Pragma("unroll") for (int i = 0; i < 8; ++i)                                                                    \
        Vt[buf][i] = ld16<AUX_NT>(rv, voff + (i & 3) * 1024, so_ + (i >> 2) * 4096);                                 \
    PIN();                                                                                                           \
  } while (0)

  if (streamer) {
    const int to = lane == 0 ? 0 : kDrop;
    const unsigned t0 = __builtin_amdgcn_raw_buffer_load_b32(rk, to, wave * 131072, AUX_NT);
    const unsigned t1 = __builtin_amdgcn_raw_buffer_load_b32(rv, to, wave * 131072, AUX_NT);
    const unsigned t2 = __builtin_amdgcn_raw_buffer_load_b32(ro, to, wave * 16384, AUX_NT);
    asm volatile("" ::"v"(t0), "v"(t1), "v"(t2));
    const v4u s4 = {kSent, kSent, kSent, kSent};
    __builtin_amdgcn_raw_buffer_store_b128(s4, rws, WS_OP + wg * 8192 + tid * 16, 0, AUX_SC1);
    __builtin_amdgcn_raw_buffer_store_b128(s4, rws, WS_OP + wg * 8192 + 4096 + tid * 16, 0, AUX_SC1);
    __builtin_amdgcn_raw_buffer_store_b128(s4, rws, WS_PART + wg * kPartSlot + tid * 16, 0, AUX_SC1);
    asm volatile("s_waitcnt vmcnt(0)" ::: "memory");
    lds_arrive(cnt + C_RESET, lane);
  } else {
    const int xw = wave - 4;
    if (lane < 32) *reinterpret_cast<v4u*>(lds + L_X + xw * 512 + lane * 16) = xv;
    asm volatile("s_waitcnt lgkmcnt(0)" ::: "memory");
    float qa[16];
#pragma unroll
    for (int i = 0; i < 16; ++i) qa[i] = 0.f;
#pragma unroll
    for (int h = 0; h < 2; ++h) {
      const v4u x0 = *reinterpret_cast<const v4u*>(lds + L_X + xw * 512 + (lane >> 3) * 32 + h * 16);
      const v4u x1 = *reinterpret_cast<const v4u*>(lds + L_X + xw * 512 + 256 + (lane >> 3) * 32 + h * 16);
#pragma unroll
      for (int e = 0; e < 8; ++e) {
        const v4u c = column_of(wqr + 8 * h, e);
#pragma unroll
        for (int u = 0; u < 4; ++u) {
          qa[e] = dot2(c[u], x0[u], qa[e]);
          qa[8 + e] = dot2(c[u], x1[u], qa[8 + e]);
        }
      }
    }
    float r8[8], r4[4];
#pragma unroll
    for (int e = 0; e < 8; ++e) r8[e] = swap32_add(qa[e], qa[8 + e]);
#pragma unroll
    for (int e = 0; e < 4; ++e) r4[e] = swap16_add(r8[e], r8[4 + e]);
    const bool up = lane & 8;
    const float o0 = (up ? r4[2] : r4[0]) + dpp<ROR8>(up ? r4[0] : r4[2]);
    const float o1 = (up ? r4[3] : r4[1]) + dpp<ROR8>(up ? r4[1] : r4[3]);
    *reinterpret_cast<float2*>(lds + L_QRED + xw * 512 + lane * 8) = make_float2(o0, o1);
    lds_arrive(cnt + C_QPART, lane);
  }
  if (wave == 7) {
    const u64 epoch = ((u64)(unsigned)__builtin_amdgcn_readfirstlane(ep[1]) << 32) | (unsigned)__builtin_amdgcn_readfirstlane(ep[0]);
    const u64 tg = mix64(epoch);
    const unsigned t8 = (unsigned)tg & 0xFF000000u, th = (unsigned)(tg >> 32);
    if (lane == 0) *reinterpret_cast<v4u*>(lds + L_TAG) = v4u{(unsigned)epoch, (unsigned)(epoch >> 32), t8, th};
    lds_wait(cnt + C_QPART, 4u);
    float s0 = 0.f, s1 = 0.f;
#pragma unroll
    for (int w = 0; w < 4; ++w) {
      const float2 q = *reinterpret_cast<const float2*>(lds + L_QRED + w * 512 + lane * 8);
      s0 += q.x;
      s1 += q.y;
    }
    const v4u g = {t8 | (__float_as_uint(s0) >> 8), th, t8 | (__float_as_uint(s1) >> 8), th};
    const int col = 8 * (lane & 7) + 4 * ((lane >> 4) & 1) + 2 * ((lane >> 3) & 1);
    __builtin_amdgcn_raw_buffer_store_b128(g, rws, WS_QG + (((pair * 2 + (lane >> 5)) * 2 + kh) * 1024 + qblk * 64 + col) * 8, 0, AUX_SC1);
    const unsigned t_own = t_gate - kT0;
    __builtin_amdgcn_raw_buffer_store_b64(v2u{t8 | (t_own & 0xFFFFFFu), th}, rws, lane == 0 ? WS_TIN + wg * 8 : kDrop, 0, AUX_SC1);
    lds_wait(cnt + C_RESET, 4u);
    __builtin_amdgcn_raw_buffer_store_b64(v2u{t8, th}, rws, lane == 0 ? WS_RDY + wg * 8 : kDrop, 0, AUX_SC1);
    wait_until(t_own + kTGather);
    const int qg_off = WS_QG + (pair * 2 + b) * 16384 + lane * 16;
    v4u qg[16];
    v2u tin;
    unsigned spins = 0, bad;
    do {
      MEMFENCE();
#pragma unroll
      for (int i = 0; i < 16; ++i) qg[i] = ld16<AUX_SC1>(rws, qg_off, i * 1024);
      tin = __builtin_amdgcn_raw_buffer_load_b64(rws, WS_TIN + pair * 256 + (lane & 31) * 8, 0, AUX_SC1);
      unsigned bl = tin[0] ^ t8, bh = tin[1] ^ th;
#pragma unroll
      for (int i = 0; i < 16; ++i) {
        bl |= (qg[i][0] ^ t8) | (qg[i][2] ^ t8);
        bh |= (qg[i][1] ^ th) | (qg[i][3] ^ th);
      }
      bad = (bl >> 24) | bh;
    } while (__any(bad != 0u) && ++spins < kSpinCap);
#pragma unroll
    for (int i = 0; i < 8; ++i)
      *reinterpret_cast<unsigned*>(lds + L_QIMG + i * 256 + lane * 4) =
          pack_bf16(__uint_as_float(qg[i][0] << 8) + __uint_as_float(qg[8 + i][0] << 8),
                    __uint_as_float(qg[i][2] << 8) + __uint_as_float(qg[8 + i][2] << 8));
    int lead = (int)((t_own - tin[0]) << 8) >> 8;
    lead = max(lead, __builtin_amdgcn_mov_dpp(lead, XOR1, 0xf, 0xf, true));
    lead = max(lead, __builtin_amdgcn_mov_dpp(lead, XOR2, 0xf, 0xf, true));
    lead = max(lead, __builtin_amdgcn_mov_dpp(lead, ROR4, 0xf, 0xf, true));
    lead = max(lead, __builtin_amdgcn_mov_dpp(lead, ROR8, 0xf, 0xf, true));
    lead = max(__builtin_amdgcn_readlane(lead, 0), __builtin_amdgcn_readlane(lead, 16));
    if (lane == 0) cnt[C_T0] = t_own - (unsigned)min(max(lead, 0), 1000) - (unsigned)((wg * 37) & 31);
    lds_arrive(cnt + C_QIMG, lane);
  }
  PIN();
  LOAD_K(0, 0);
  LOAD_V(0, 0);
  LOAD_K(1, 0);
  LOAD_V(1, 0);
  lds_wait<1>(cnt + C_QIMG, 1u);
  t_gate = __builtin_amdgcn_readfirstlane(cnt[C_T0]) + kT0;
  const v4u tagv = *reinterpret_cast<const v4u*>(lds + L_TAG);
  const int pp = (lane >> 3) & 1, hh = lane & 7, gg = lane >> 4;
  const v4u Q0 = *reinterpret_cast<const v4u*>(lds + L_QIMG + hh * 256 + pp * 64 + gg * 16);
  const v4u Q1 = *reinterpret_cast<const v4u*>(lds + L_QIMG + hh * 256 + 128 + pp * 64 + gg * 16);

  f32x4 acc[8];
#pragma unroll
  for (int t = 0; t < 8; ++t) acc[t] = f32x4{0.f, 0.f, 0.f, 0.f};
  float m = -INFINITY, l = 0.f;
  const unsigned pmask = pp ? 0xFFFF0000u : 0x0000FFFFu;
#define TILE(buf, AFTER_QK, AFTER_PV)                                                                                \
  do {                                                                                                               \
    float s[4];                                                                                                      \
    _Pragma("unroll") for (int T = 0; T < 4; ++T) {                                                                  \
      f32x4 d = mfma16(Kt[buf][2 * T], Q0, f32x4{0.f, 0.f, 0.f, 0.f});                                               \
      d = mfma16(Kt[buf][2 * T + 1], Q1, d);                                                                         \
      s[T] = (pp ? d[3] : d[0]) + dpp<ROR8>(pp ? d[1] : d[2]);                                                       \
    }                                                                                                                \
    PIN();                                                                                                           \
    AFTER_QK;                                                                                                        \
    float mt = fmaxf(fmaxf(s[0], s[1]), fmaxf(s[2], s[3]));                                                          \
    mt = fmaxf(mt, dpp<ROR8>(mt));                                                                                   \
    mt = swap32_max(swap16_max(mt)) * kC2;                                                                           \
    const float mn = fmaxf(m, mt);                                                                                   \
    if (__any(mn != m)) {                                                                                            \
      const float alpha = __builtin_amdgcn_exp2f(m - mn);                                                            \
      l *= alpha;                                                                                                    \
      _Pragma("unroll") for (int i = 0; i < 8; ++i) acc[i] *= alpha;                                                 \
      m = mn;                                                                                                        \
    }                                                                                                                \
    v4u P;                                                                                                           \
    _Pragma("unroll") for (int T = 0; T < 4; ++T) {                                                                  \
      const float e = __builtin_amdgcn_exp2f(fmaf(s[T], kC2, -m));                                                   \
      l += e;                                                                                                        \
      P[T] = pack_bf16(e, e) & pmask;                                                                                \
    }                                                                                                                \
    _Pragma("unroll") for (int i = 0; i < 8; ++i) acc[i] = mfma16(column_of(Vt[buf], i), P, acc[i]);                 \
    PIN();                                                                                                           \
    AFTER_PV;                                                                                                        \
  } while (0)
  const int nunit = streamer ? kUnitsS : 1;
#pragma unroll 1
  for (int u = 1; u < nunit; ++u) {
    TILE(0, LOAD_K(0, u), LOAD_V(0, u));
    TILE(1, LOAD_K(1, u), LOAD_V(1, u));
  }
  v4u rdy = ld16<AUX_SC1>(rws, wave < 2 ? lane * 16 : kDrop, WS_RDY + br * 1024);
  PIN();
  if (streamer) lds_arrive(cnt + C_LAST, lane);
  PIN();
  TILE(0, PIN(), PIN());
  TILE(1, PIN(), PIN());
#undef TILE
#undef LOAD_K
#undef LOAD_V

  l += dpp<ROR8>(l);
  l = swap16_add(l, l);
  l = swap32_add(l, l);
  {
    float* dst = reinterpret_cast<float*>(lds + L_MRG) + (wave * 8 + hh) * 132 + 32 * gg + 16 * pp;
#pragma unroll
    for (int rr = 0; rr < 2; ++rr)
#pragma unroll
      for (int q = 0; q < 2; ++q) {
        f32x4 o;
#pragma unroll
        for (int e = 0; e < 4; ++e) {
          const f32x4 a = acc[4 * q + e];
          o[e] = (pp ? a[2 + rr] : a[rr]) + dpp<ROR8>(pp ? a[rr] : a[2 + rr]);
        }
        *reinterpret_cast<f32x4*>(dst + 8 * rr + 4 * q) = o;
      }
    if (lane < 8) {
      float* ml = reinterpret_cast<float*>(lds + L_MRGML) + lane * 16;
      ml[wave] = m;
      ml[8 + wave] = l;
    }
  }
  lds_arrive(cnt + C_PART, lane);
  if (streamer) {
    lds_wait(cnt + C_PART, 8u);
    {
      const int hd = 2 * wave + (lane >> 5);
      const float* ml = reinterpret_cast<const float*>(lds + L_MRGML) + hd * 16;
      const f32x4 m0 = *reinterpret_cast<const f32x4*>(ml), m1 = *reinterpret_cast<const f32x4*>(ml + 4);
      const f32x4 l0 = *reinterpret_cast<const f32x4*>(ml + 8), l1 = *reinterpret_cast<const f32x4*>(ml + 12);
      f32x4 o[8];
#pragma unroll
      for (int w = 0; w < 8; ++w) o[w] = *reinterpret_cast<const f32x4*>(lds + L_MRG + ((w * 8 + hd) * 132 + 4 * (lane & 31)) * 4);
      const float M = fmaxf(fmaxf(fmaxf(m0[0], m0[1]), fmaxf(m0[2], m0[3])), fmaxf(fmaxf(m1[0], m1[1]), fmaxf(m1[2], m1[3])));
      float L = 0.f;
      f32x4 os = {0.f, 0.f, 0.f, 0.f};
#pragma unroll
      for (int w = 0; w < 8; ++w) {
        const float e = __builtin_amdgcn_exp2f((w < 4 ? m0[w & 3] : m1[w & 3]) - M);
        L = fmaf(e, w < 4 ? l0[w & 3] : l1[w & 3], L);
        os += e * o[w];
      }
      const float inv = __builtin_amdgcn_rcpf(L);
      const unsigned u0 = quiet(os[0] * inv), u1 = quiet(os[1] * inv), u2 = quiet(os[2] * inv), u3 = quiet(os[3] * inv);
      const v4u ov = {quiet(M + __builtin_amdgcn_logf(L)), __builtin_amdgcn_perm(u0, u1, 0x07060501u),
                      __builtin_amdgcn_perm(u2, u1, 0x07060302u), __builtin_amdgcn_perm(u3, u2, 0x07060501u)};
      __builtin_amdgcn_raw_buffer_store_b128(ov, rws, WS_PART + wg * kPartSlot + wave * 1024 + lane * 16, 0, AUX_SC1);
    }
    PIN();
    if (wave < 2) {
#define RDY_BAD() __any(((rdy[0] ^ tagv[2]) | (rdy[1] ^ tagv[3]) | (rdy[2] ^ tagv[2]) | (rdy[3] ^ tagv[3])) != 0u)
      if (RDY_BAD()) {
        unsigned spins = 0;
        do {
          nap(1);
          rdy = ld16<AUX_SC1>(rws, lane * 16, WS_RDY + br * 1024);
        } while (RDY_BAD() && ++spins < kSpinCap);
      }
#undef RDY_BAD
      const int po = WS_PART + (pair * 32 + wave * 16 + (lane >> 3)) * kPartSlot + idx * 128 + 16 * (lane & 7);
      nap(kNap1);
      v4u pa = ld16<AUX_SC1>(rws, po, 0), pb = ld16<AUX_SC1>(rws, po, 8 * kPartSlot);
      for (unsigned spins = 0;; ++spins) {
        const bool ok = max(umax4(pa), umax4(pb)) != kSent;
        if (__all(ok) || spins > kSpinCap) break;
        nap(kNapPoll);
        if (!ok) {
          pa = ld16<AUX_SC1>(rws, po, 0);
          pb = ld16<AUX_SC1>(rws, po, 8 * kPartSlot);
        }
      }
      const float la = __uint_as_float(pa[0]), lb = __uint_as_float(pb[0]);
      float M = fmaxf(la, lb);
      M = fmaxf(M, dpp<ROR8>(M));
      M = swap32_max(swap16_max(M));
      const float ea = __builtin_amdgcn_exp2f(la - M), eb = __builtin_amdgcn_exp2f(lb - M);
      float v[5] = {ea + eb,
                    ea * __uint_as_float(pa[1] & 0xFFFFFF00u) + eb * __uint_as_float(pb[1] & 0xFFFFFF00u),
                    ea * __uint_as_float(__builtin_amdgcn_perm(pa[2], pa[1], 0x0504000Cu)) +
                        eb * __uint_as_float(__builtin_amdgcn_perm(pb[2], pb[1], 0x0504000Cu)),
                    ea * __uint_as_float(__builtin_amdgcn_perm(pa[2], pa[3], 0x0706000Cu)) +
                        eb * __uint_as_float(__builtin_amdgcn_perm(pb[2], pb[3], 0x0706000Cu)),
                    ea * __uint_as_float(pa[3] & 0xFFFFFF00u) + eb * __uint_as_float(pb[3] & 0xFFFFFF00u)};
#pragma unroll
      for (int i = 0; i < 5; ++i) v[i] += dpp<ROR8>(v[i]);
#pragma unroll
      for (int i = 0; i < 5; ++i) v[i] = swap16_add(v[i], v[i]);
#pragma unroll
      for (int i = 0; i < 5; ++i) v[i] = swap32_add(v[i], v[i]);
      const float inv = __builtin_amdgcn_rcpf(v[0]);
      const v2u ob = {pack_bf16(v[1] * inv, v[2] * inv), pack_bf16(v[3] * inv, v[4] * inv)};
      if (lane < 8) *reinterpret_cast<v2u*>(lds + L_OIMG + wave * 64 + lane * 8) = ob;
    }
    wg_barrier();
    if (wave != 0) return;
  } else {
    lds_wait<4>(cnt + C_LAST, 3u);
    PIN();
    v4u wor[16], wc[16];
#pragma unroll
    for (int i = 0; i < 16; ++i) {
      if ((i & 7) == 0) GATE(false);
      wor[i] = ld16<AUX_NT>(ro, gg * 8 * 2048 + (lane & 15) * 16, (i & 7) * 2048 + (i >> 3) * 256 + (wave & 3) * 512);
    }
    PIN();
#pragma unroll
    for (int e = 0; e < 16; ++e) {
      wc[e] = column_of(wor + (e & 8), e & 7);
      asm volatile("" : "+v"(wc[e]));
    }
    int ba[8];
#pragma unroll
    for (int e = 0; e < 8; ++e) {
      ba[e] = ((lane & 15) >> 1) == e ? L_OIMG + (lane & 1) * 64 + gg * 16 : L_ZERO;
      asm volatile("" : "+v"(ba[e]));
    }
    wg_barrier();
    v4u bf[8];
#pragma unroll
    for (int e = 0; e < 8; ++e) bf[e] = *reinterpret_cast<const v4u*>(lds + ba[e]);
    f32x4 d0 = {0.f, 0.f, 0.f, 0.f}, d1 = {0.f, 0.f, 0.f, 0.f};
#pragma unroll
    for (int e = 0; e < 8; ++e) {
      d0 = mfma16(wc[e], bf[e], d0);
      d1 = mfma16(wc[8 + e], bf[e], d1);
    }
    const v4u o0 = {not_sentinel(d0[0]), not_sentinel(d0[1]), not_sentinel(d0[2]), not_sentinel(d0[3])};
    const v4u o1 = {not_sentinel(d1[0]), not_sentinel(d1[1]), not_sentinel(d1[2]), not_sentinel(d1[3])};
    const int so = WS_OP + wg * 8192 + (wave & 3) * 2048 + lane * 16;
    __builtin_amdgcn_raw_buffer_store_b128(o0, rws, so, 0, AUX_SC1);
    __builtin_amdgcn_raw_buffer_store_b128(o1, rws, so + 1024, 0, AUX_SC1);
    return;
  }
#undef GATE

  const int r = wg & 127;
  const int fo = WS_OP + (br * 128 + (lane >> 2)) * 8192 + r * 64 + (lane & 3) * 16;
  const int pos = 4 * r + (lane & 3), ln = pos & 63;
  unsigned short* dst = p.out + (ln & 1) * 2048 + br * 1024 + 128 * (pos >> 6) + 32 * (ln >> 4) + 8 * (lane >> 4) + ((ln & 15) >> 1);
  asm volatile("" : "+v"(dst));
  v4u f[8];
  nap(kNap2);
#pragma unroll
  for (int q = 0; q < 8; ++q) f[q] = ld16<AUX_SC1>(rws, fo, q * 16 * 8192);
  for (unsigned spins = 0;; ++spins) {
    unsigned mx = 0u;
#pragma unroll
    for (int q = 0; q < 8; ++q) mx = max(mx, umax4(f[q]));
    const bool ok = mx != kSent;
    if (__all(ok) || spins > kSpinCap) break;
    nap(kNapPoll);
    if (!ok) {
#pragma unroll
      for (int q = 0; q < 8; ++q) f[q] = ld16<AUX_SC1>(rws, fo, q * 16 * 8192);
    }
  }
  float y[4];
#pragma unroll
  for (int e = 0; e < 4; ++e)
    y[e] = ((__uint_as_float(f[0][e]) + __uint_as_float(f[1][e])) + (__uint_as_float(f[2][e]) + __uint_as_float(f[3][e]))) +
           ((__uint_as_float(f[4][e]) + __uint_as_float(f[5][e])) + (__uint_as_float(f[6][e]) + __uint_as_float(f[7][e])));
  const float z0 = swap32_add(y[0], y[2]), z1 = swap32_add(y[1], y[3]);
  float z = swap16_add(z0, z1);
  z += dpp<ROR4>(z);
  z += dpp<ROR8>(z);
  if ((lane & 12) == 0) *(__attribute__((address_space(1))) unsigned short*)dst = (unsigned short)(pack_bf16(z, z) & 0xFFFFu);
  if (r == 0 && lane == 0) {
    const u64 nx = (((u64)tagv[1] << 32) | tagv[0]) + 1;
    __builtin_amdgcn_raw_buffer_store_b64(v2u{(unsigned)nx, (unsigned)(nx >> 32)}, rws, WS_EPOCH + br * 256, 0, AUX_SC1);
  }
}

}

extern "C" void kernel_launch(void* const* d_in, const int* in_sizes, int n_in, void* d_out, int out_size, void* d_ws,
                              size_t ws_size, hipStream_t stream) {
  (void)in_sizes; (void)n_in; (void)out_size; (void)ws_size;
  Params p;
  for (int i = 0; i < 2; ++i) {
    p.x[i] = (const unsigned short*)d_in[5 * i + 0];
    p.k[i] = (const unsigned short*)d_in[5 * i + 1];
    p.v[i] = (const unsigned short*)d_in[5 * i + 2];
    p.wq[i] = (const unsigned short*)d_in[5 * i + 3];
    p.wo[i] = (const unsigned short*)d_in[5 * i + 4];
  }
  p.out = (unsigned short*)d_out;
  p.ws = (unsigned char*)d_ws;
  hipLaunchKernelGGL(fused_decode, dim3(kNWG), dim3(kNThr), 0, stream, p);
}
```

```cpp
#include <hip/hip_runtime.h>
#include <hip/hip_bf16.h>
#include <stdint.h>

typedef unsigned v4u __attribute__((ext_vector_type(4)));
typedef unsigned v2u __attribute__((ext_vector_type(2)));
typedef float f32x4 __attribute__((ext_vector_type(4)));
typedef __bf16 bf16x8 __attribute__((ext_vector_type(8)));
typedef __bf16 bf16x2 __attribute__((ext_vector_type(2)));
typedef unsigned long long u64;
typedef __amdgpu_buffer_rsrc_t rsrc_t;

namespace {

constexpr int kD = 1024, kHkv = 4, kSk = 32768, kDh = 128;
constexpr int kNWG = 256, kNThr = 512;
constexpr int kUnitsS = 7;
constexpr int AUX_NT = 2, AUX_SC1 = 16;
constexpr int RSRC_W3 = 0x00020000;
constexpr unsigned kSent = 0xFFFFFFFFu;
constexpr int kDrop = 0x7FFFFF00;
constexpr unsigned kSpinCap = 1u << 18;
constexpr float kC2 = 0.088388347648318440f * 1.4426950408889634f;

#define SCHED_TAU 450
constexpr unsigned kT0 = 400;
constexpr unsigned kTau16 = SCHED_TAU;
constexpr int kWin16 = 48, kWinGain = 51;
constexpr int kLate = 100;
constexpr unsigned kMark1 = 44, kMark2 = 68;
constexpr unsigned kMarkWait = 250;
constexpr float kSlowRatio = 1.02f, kSlowLag = 145.f, kSlowMargin = 1.005f, kSlowMin = 475.f / 16.f, kSlowMax = 520.f / 16.f;
constexpr float kSlowBack = 60.f;
constexpr unsigned kTGather = 540;
constexpr unsigned kNap1 = 70, kNap2 = 95, kNapPoll = 20;

constexpr int WS_EPOCH = 0;
constexpr int WS_RDY = 2048;
constexpr int WS_TIN = 4096;
constexpr int WS_LAG = 6144;
constexpr int WS_QG = 8192;
constexpr int kPartSlot = 4096;
constexpr int WS_PART = WS_QG + 8 * 2 * 2 * 1024 * 8;
constexpr int WS_OP = WS_PART + 256 * kPartSlot;
constexpr int WS_END = WS_OP + 256 * 8192;

constexpr int L_X = 0;
constexpr int L_TAG = L_X + 2048;
constexpr int L_CNT = L_TAG + 16;
constexpr int L_QRED = L_CNT + 144;
constexpr int L_QIMG = L_QRED + 4 * 512;
constexpr int L_OIMG = L_QIMG + 2048;
constexpr int L_ZERO = L_OIMG + 128;
constexpr int L_MRGML = L_ZERO + 16;
constexpr int L_MRG = L_MRGML + 8 * 16 * 4;
constexpr int L_END = L_MRG + 8 * 8 * 132 * 4;

enum {
  C_QPART, C_QIMG, C_RESET, C_LAST, C_PART,
  C_TICKET, C_LANDED, C_ANCHOR,
  C_PROG = 8,
  C_DUE0 = 12, C_SLOPE, C_MARK1, C_MARK2,
  C_RING = 16,
  C_RINGPOS = 32,
  C_WORDS
};

struct Params {
  const unsigned short* x[2];
  const unsigned short* k[2];
  const unsigned short* v[2];
  const unsigned short* wq[2];
  const unsigned short* wo[2];
  unsigned short* out;
  unsigned char* ws;
};

__device__ __forceinline__ rsrc_t make_rsrc(const void* p, int bytes) {
  return __builtin_amdgcn_make_buffer_rsrc((void*)p, 0, bytes, RSRC_W3);
}
template <int AUX> __device__ __forceinline__ v4u ld16(rsrc_t r, int voff, int soff) {
  return __builtin_amdgcn_raw_buffer_load_b128(r, voff, soff, AUX);
}
__device__ __forceinline__ f32x4 mfma16(v4u a, v4u b, f32x4 c) {
  return __builtin_amdgcn_mfma_f32_16x16x32_bf16(__builtin_bit_cast(bf16x8, a), __builtin_bit_cast(bf16x8, b), c, 0, 0, 0);
}
__device__ __forceinline__ float dot2(unsigned a, unsigned b, float c) {
  return __builtin_amdgcn_fdot2_f32_bf16(__builtin_bit_cast(bf16x2, a), __builtin_bit_cast(bf16x2, b), c, false);
}
__device__ __forceinline__ unsigned pack_bf16(float lo, float hi) {
  bf16x2 r = {(__bf16)lo, (__bf16)hi};
  return __builtin_bit_cast(unsigned, r);
}
template <int CTRL> __device__ __forceinline__ float dpp(float x) {
  return __builtin_bit_cast(float, __builtin_amdgcn_mov_dpp(__builtin_bit_cast(int, x), CTRL, 0xf, 0xf, true));
}
constexpr int XOR1 = 0xB1, XOR2 = 0x4E, ROR4 = 0x124, ROR8 = 0x128;
__device__ __forceinline__ float swap16_add(float a, float b) {
  auto s = __builtin_amdgcn_permlane16_swap(__float_as_uint(a), __float_as_uint(b), false, false);
  return __uint_as_float(s[0]) + __uint_as_float(s[1]);
}
__device__ __forceinline__ float swap32_add(float a, float b) {
  auto s = __builtin_amdgcn_permlane32_swap(__float_as_uint(a), __float_as_uint(b), false, false);
  return __uint_as_float(s[0]) + __uint_as_float(s[1]);
}
__device__ __forceinline__ float swap16_max(float a) {
  auto s = __builtin_amdgcn_permlane16_swap(__float_as_uint(a), __float_as_uint(a), false, false);
  return fmaxf(__uint_as_float(s[0]), __uint_as_float(s[1]));
}
__device__ __forceinline__ float swap32_max(float a) {
  auto s = __builtin_amdgcn_permlane32_swap(__float_as_uint(a), __float_as_uint(a), false, false);
  return fmaxf(__uint_as_float(s[0]), __uint_as_float(s[1]));
}
__device__ __forceinline__ v4u column_of(const v4u* r, int t) {
  v4u o;
#pragma unroll
  for (int u = 0; u < 4; ++u)
    o[u] = __builtin_amdgcn_perm(r[2 * u + 1][t >> 1], r[2 * u][t >> 1], (t & 1) ? 0x07060302u : 0x05040100u);
  return o;
}
__device__ __forceinline__ unsigned not_sentinel(float f) { return min(__float_as_uint(f), kSent - 1u); }
__device__ __forceinline__ unsigned quiet(float f) { return f != f ? 0x7FC00000u : __float_as_uint(f); }
__device__ __forceinline__ unsigned umax4(v4u a) { return max(max(a[0], a[1]), max(a[2], a[3])); }
__device__ __forceinline__ u64 mix64(u64 z) {
  z += 0x9E3779B97F4A7C15ull;
  z = (z ^ (z >> 30)) * 0xBF58476D1CE4E5B9ull;
  z = (z ^ (z >> 27)) * 0x94D049BB133111EBull;
  return z ^ (z >> 31);
}
#define PIN() __builtin_amdgcn_sched_barrier(0)
#define MEMFENCE() asm volatile("" ::: "memory")
__device__ __forceinline__ void wg_barrier() {
  asm volatile("s_waitcnt lgkmcnt(0)" ::: "memory");
  __builtin_amdgcn_s_barrier();
  MEMFENCE();
}
__device__ __forceinline__ void lds_arrive(unsigned* c, int lane) {
  asm volatile("s_waitcnt lgkmcnt(0)" ::: "memory");
  if (lane == 0) __hip_atomic_fetch_add(c, 1u, __ATOMIC_RELAXED, __HIP_MEMORY_SCOPE_WORKGROUP);
}
template <int SLEEP = 0> __device__ __forceinline__ void lds_wait(unsigned* c, unsigned n) {
  while (__hip_atomic_load(c, __ATOMIC_RELAXED, __HIP_MEMORY_SCOPE_WORKGROUP) < n) __builtin_amdgcn_s_sleep(SLEEP);
  MEMFENCE();
}
__device__ __forceinline__ unsigned now_ticks() { return (unsigned)__builtin_amdgcn_s_memrealtime(); }
__device__ __forceinline__ void wait_until(unsigned t) {
  while ((int)(now_ticks() - t) < 0) __builtin_amdgcn_s_sleep(1);
  MEMFENCE();
}
__device__ __forceinline__ void nap(unsigned ticks) { wait_until(now_ticks() + ticks); }
__device__ __forceinline__ unsigned lds_get(unsigned* c) {
  return __hip_atomic_load(c, __ATOMIC_RELAXED, __HIP_MEMORY_SCOPE_WORKGROUP);
}
__device__ __forceinline__ void lds_put(unsigned* c, unsigned v) {
  __hip_atomic_store(c, v, __ATOMIC_RELAXED, __HIP_MEMORY_SCOPE_WORKGROUP);
}
__device__ __forceinline__ unsigned uni(unsigned v) { return __builtin_amdgcn_readfirstlane(v); }

__global__ void __launch_bounds__(kNThr, 2) fused_decode(Params p) {
  __shared__ __attribute__((aligned(16))) unsigned char lds[L_END];
  const int tid = threadIdx.x, lane = tid & 63;
  const int wave = __builtin_amdgcn_readfirstlane(tid >> 6);
  const bool streamer = wave < 4;
  const int wg = blockIdx.x, pair = wg >> 5, idx = wg & 31;
  const int br = pair >> 2, kvh = pair & 3, b = idx >> 4, split = idx & 15;
  const int qblk = idx & 15, kh = idx >> 4;
  const unsigned t_in = now_ticks();
  PIN();
  const rsrc_t rws = make_rsrc(p.ws, WS_END);
  const rsrc_t ro = make_rsrc(p.wo[br] + (size_t)(kvh * 1024 + idx * 32) * kD, 32 * kD * 2);
  const size_t kv_off = ((size_t)(b * kHkv + kvh) * kSk + (size_t)split * 2048) * kDh;
  const rsrc_t rk = make_rsrc(p.k[br] + kv_off, 2048 * kDh * 2);
  const rsrc_t rv = make_rsrc(p.v[br] + kv_off, 2048 * kDh * 2);
  const int toff0 = ((wave + wg + (wg >> 3) - (streamer ? 0 : 4)) & 7) * 8192, toff1 = toff0 ^ 32768;
  const int blk0 = (wg >> 3) + (wg >> 6) * 3 + (streamer ? 0 : 7);
  const unsigned phase = (wg * 37) & 31;

  unsigned* const cnt = reinterpret_cast<unsigned*>(lds + L_CNT);
  v4u wqr[16], xv;
  v2u ep;
  if (!streamer) {
    const int xw = wave - 4;
    const rsrc_t rq = make_rsrc(p.wq[br] + (size_t)(kh * 512 + xw * 128) * 4096 + kvh * 1024 + qblk * 64, 127 * 8192 + 128);
    const rsrc_t rx = make_rsrc(p.x[br] + kh * 512 + xw * 128, 2048 + 256);
    const int qo = (lane >> 3) * 16 * 8192 + (lane & 7) * 16;
    ep = __builtin_amdgcn_raw_buffer_load_b64(rws, wave == 7 ? WS_EPOCH + br * 256 : kDrop, 0, AUX_SC1);
    xv = ld16<0>(rx, lane < 32 ? (lane >> 4) * 2048 + (lane & 15) * 16 : kDrop, 0);
#pragma unroll
    for (int i = 0; i < 16; ++i) wqr[i] = ld16<AUX_NT>(rq, qo, i * 8192);
    if (tid < 256 + C_WORDS) cnt[lane] = lane == C_ANCHOR ? t_in : lane == C_DUE0 ? t_in + kT0 : lane == C_SLOPE ? 4u * kTau16 : 0u;
    if (tid == 320) *reinterpret_cast<v4u*>(lds + L_ZERO) = v4u{0u, 0u, 0u, 0u};
  }
  PIN();
  wg_barrier();
  PIN();
  const int kl = (lane & 15) >> 1;
  int koff = (4 * (kl & 1) + (kl >> 1)) * 256 + (4 * (lane & 1) + (lane >> 4)) * 16;
  int voff = lane * 16;
  v4u Kt[2][8], Vt[2][8];
  bool owes = false;
  unsigned ngrp = 0;
#define REPORT()                                                                                                     \
  do {                                                                                                               \
    if (lane == 0) {                                                                                                 \
      const unsigned i_ = __hip_atomic_fetch_add(cnt + C_RINGPOS, 1u, __ATOMIC_RELAXED, __HIP_MEMORY_SCOPE_WORKGROUP); \
      lds_put(cnt + C_RING + (i_ & 15u), now_ticks());                                                               \
    }                                                                                                                \
    lds_arrive(cnt + C_LANDED, lane);                                                                                \
    owes = false;                                                                                                    \
  } while (0)
#define GATE()                                                                                                       \
  do {                                                                                                               \
    if (streamer) {                                                                                                  \
      for (int w_ = 0; w_ < 200; ++w_) {                                                                             \
        const unsigned a_ = min(min(lds_get(cnt + C_PROG), lds_get(cnt + C_PROG + 1)),                               \
                                min(lds_get(cnt + C_PROG + 2), lds_get(cnt + C_PROG + 3)));                          \
        if (ngrp <= uni(a_)) break;                                                                                  \
        __builtin_amdgcn_s_sleep(1);                                                                                 \
      }                                                                                                              \
      ++ngrp;                                                                                                        \
      if (lane == 0) lds_put(cnt + C_PROG + wave, ngrp);                                                             \
    }                                                                                                                \
    unsigned tk_ = 0;                                                                                                \
    if (lane == 0) tk_ = __hip_atomic_fetch_add(cnt + C_TICKET, 1u, __ATOMIC_RELAXED, __HIP_MEMORY_SCOPE_WORKGROUP); \
    tk_ = uni(tk_);                                                                                                  \
    const unsigned d0_ = lds_get(cnt + C_DUE0), sl_ = uni(lds_get(cnt + C_SLOPE));                                   \
    const unsigned due_ = uni(d0_) + ((tk_ * sl_) >> 6);                                                             \
    if (owes) {                                                                                                      \
      if ((int)(now_ticks() - due_) < kLate) asm volatile("s_waitcnt vmcnt(0)" ::: "memory");                        \
      else asm volatile("s_waitcnt vmcnt(4)" ::: "memory");                                                          \
      REPORT();                                                                                                      \
    }                                                                                                                \
    owes = true;                                                                                                     \
    for (;;) {                                                                                                       \
      const unsigned now_ = now_ticks();                                                                             \
      const int late_ = (int)(now_ - due_);                                                                          \
      if (late_ >= 0) {                                                                                              \
        const int c16_ = kWin16 + ((late_ * kWinGain) >> 8);                                                         \
        const int n_ = (c16_ + 15) >> 4, need_ = (int)tk_ - n_;                                                      \
        if (need_ < 0) break;                                                                                        \
        const int landed_ = (int)uni(lds_get(cnt + C_LANDED));                                                       \
        if (landed_ > need_ + 1) break;                                                                              \
        if (landed_ > need_ &&                                                                                       \
            (int)(now_ - uni(lds_get(cnt + C_RING + (need_ & 15)))) >= (int)(((n_ * 16 - c16_) * sl_) >> 10))        \
          break;                                                                                                     \
      }                                                                                                              \
      __builtin_amdgcn_s_sleep(1);                                                                                   \
    }                                                                                                                \
    if ((tk_ == kMark1 || tk_ == kMark2) && lane == 0) lds_put(cnt + (tk_ == kMark1 ? C_MARK1 : C_MARK2), now_ticks() | 1u); \
    MEMFENCE();                                                                                                      \
  } while (0)
#define SETTLE()                                                                                                     \
  do {                                                                                                               \
    if (owes) {                                                                                                      \
      asm volatile("s_waitcnt vmcnt(0)" ::: "memory");                                                               \
      REPORT();                                                                                                      \
    }                                                                                                                \
  } while (0)
#define LOAD_K(buf, u)                                                                                               \
  do {                                                                                                               \
    GATE();                                                                                                          \
    asm volatile("" : "+v"(koff));                                                                                   \
    const int so_ = ((blk0 + (u)) & 7) * 65536 + ((buf) ? toff1 : toff0);                                            \
    _Pragma("unroll") for (int i = 0; i < 8; ++i)                                                                    \
        Kt[buf][i] = ld16<AUX_NT>(rk, koff + (i & 2) * 1024 + (i & 1) * 128, so_ + (i >> 2) * 4096);                 \
    PIN();                                                                                                           \
  } while (0)
#define LOAD_V(buf, u)                                                                                               \
  do {                                                                                                               \
    GATE();                                                                                                          \
    asm volatile("" : "+v"(voff));                                                                                   \
    const int so_ = ((blk0 + (u)) & 7) * 65536 + ((buf) ? toff1 : toff0);                                            \
    _Pragma("unroll") for (int i = 0; i < 8; ++i)                                                                    \
        Vt[buf][i] = ld16<AUX_NT>(rv, voff + (i & 3) * 1024, so_ + (i >> 2) * 4096);                                 \
    PIN();                                                                                                           \
  } while (0)

  if (streamer) {
    const int to = lane == 0 ? 0 : kDrop;
    const unsigned t0 = __builtin_amdgcn_raw_buffer_load_b32(rk, to, wave * 131072, AUX_NT);
    const unsigned t1 = __builtin_amdgcn_raw_buffer_load_b32(rv, to, wave * 131072, AUX_NT);
    const unsigned t2 = __builtin_amdgcn_raw_buffer_load_b32(ro, to, wave * 16384, AUX_NT);
    asm volatile("" ::"v"(t0), "v"(t1), "v"(t2));
    const v4u s4 = {kSent, kSent, kSent, kSent};
    __builtin_amdgcn_raw_buffer_store_b128(s4, rws, WS_OP + wg * 8192 + tid * 16, 0, AUX_SC1);
    __builtin_amdgcn_raw_buffer_store_b128(s4, rws, WS_OP + wg * 8192 + 4096 + tid * 16, 0, AUX_SC1);
    __builtin_amdgcn_raw_buffer_store_b128(s4, rws, WS_PART + wg * kPartSlot + tid * 16, 0, AUX_SC1);
    asm volatile("s_waitcnt vmcnt(0)" ::: "memory");
    lds_arrive(cnt + C_RESET, lane);
  } else {
    const int xw = wave - 4;
    if (lane < 32) *reinterpret_cast<v4u*>(lds + L_X + xw * 512 + lane * 16) = xv;
    asm volatile("s_waitcnt lgkmcnt(0)" ::: "memory");
    float qa[16];
#pragma unroll
    for (int i = 0; i < 16; ++i) qa[i] = 0.f;
#pragma unroll
    for (int h = 0; h < 2; ++h) {
      const v4u x0 = *reinterpret_cast<const v4u*>(lds + L_X + xw * 512 + (lane >> 3) * 32 + h * 16);
      const v4u x1 = *reinterpret_cast<const v4u*>(lds + L_X + xw * 512 + 256 + (lane >> 3) * 32 + h * 16);
#pragma unroll
      for (int e = 0; e < 8; ++e) {
        const v4u c = column_of(wqr + 8 * h, e);
#pragma unroll
        for (int u = 0; u < 4; ++u) {
          qa[e] = dot2(c[u], x0[u], qa[e]);
          qa[8 + e] = dot2(c[u], x1[u], qa[8 + e]);
        }
      }
    }
    float r8[8], r4[4];
#pragma unroll
    for (int e = 0; e < 8; ++e) r8[e] = swap32_add(qa[e], qa[8 + e]);
#pragma unroll
    for (int e = 0; e < 4; ++e) r4[e] = swap16_add(r8[e], r8[4 + e]);
    const bool up = lane & 8;
    const float o0 = (up ? r4[2] : r4[0]) + dpp<ROR8>(up ? r4[0] : r4[2]);
    const float o1 = (up ? r4[3] : r4[1]) + dpp<ROR8>(up ? r4[1] : r4[3]);
    *reinterpret_cast<float2*>(lds + L_QRED + xw * 512 + lane * 8) = make_float2(o0, o1);
    lds_arrive(cnt + C_QPART, lane);
  }
  if (wave == 7) {
    const u64 epoch = ((u64)(unsigned)__builtin_amdgcn_readfirstlane(ep[1]) << 32) | (unsigned)__builtin_amdgcn_readfirstlane(ep[0]);
    const u64 tg = mix64(epoch);
    const unsigned t8 = (unsigned)tg & 0xFF000000u, th = (unsigned)(tg >> 32);
    if (lane == 0) *reinterpret_cast<v4u*>(lds + L_TAG) = v4u{(unsigned)epoch, (unsigned)(epoch >> 32), t8, th};
    lds_wait(cnt + C_QPART, 4u);
    float s0 = 0.f, s1 = 0.f;
#pragma unroll
    for (int w = 0; w < 4; ++w) {
      const float2 q = *reinterpret_cast<const float2*>(lds + L_QRED + w * 512 + lane * 8);
      s0 += q.x;
      s1 += q.y;
    }
    const v4u g = {t8 | (__float_as_uint(s0) >> 8), th, t8 | (__float_as_uint(s1) >> 8), th};
    const int col = 8 * (lane & 7) + 4 * ((lane >> 4) & 1) + 2 * ((lane >> 3) & 1);
    __builtin_amdgcn_raw_buffer_store_b128(g, rws, WS_QG + (((pair * 2 + (lane >> 5)) * 2 + kh) * 1024 + qblk * 64 + col) * 8, 0, AUX_SC1);
    const unsigned t_own = uni(lds_get(cnt + C_ANCHOR));
    __builtin_amdgcn_raw_buffer_store_b64(v2u{t8 | (t_own & 0xFFFFFFu), th}, rws, lane == 0 ? WS_TIN + wg * 8 : kDrop, 0, AUX_SC1);
    lds_wait(cnt + C_RESET, 4u);
    __builtin_amdgcn_raw_buffer_store_b64(v2u{t8, th}, rws, lane == 0 ? WS_RDY + wg * 8 : kDrop, 0, AUX_SC1);
    wait_until(t_own + kTGather);
    const int qg_off = WS_QG + (pair * 2 + b) * 16384 + lane * 16;
    v4u qg[16];
    v2u tin;
    unsigned spins = 0, bad;
    do {
      MEMFENCE();
#pragma unroll
      for (int i = 0; i < 16; ++i) qg[i] = ld16<AUX_SC1>(rws, qg_off, i * 1024);
      tin = __builtin_amdgcn_raw_buffer_load_b64(rws, WS_TIN + pair * 256 + (lane & 31) * 8, 0, AUX_SC1);
      unsigned bl = tin[0] ^ t8, bh = tin[1] ^ th;
#pragma unroll
      for (int i = 0; i < 16; ++i) {
        bl |= (qg[i][0] ^ t8) | (qg[i][2] ^ t8);
        bh |= (qg[i][1] ^ th) | (qg[i][3] ^ th);
      }
      bad = (bl >> 24) | bh;
    } while (__any(bad != 0u) && ++spins < kSpinCap);
#pragma unroll
    for (int i = 0; i < 8; ++i)
      *reinterpret_cast<unsigned*>(lds + L_QIMG + i * 256 + lane * 4) =
          pack_bf16(__uint_as_float(qg[i][0] << 8) + __uint_as_float(qg[8 + i][0] << 8),
                    __uint_as_float(qg[i][2] << 8) + __uint_as_float(qg[8 + i][2] << 8));
    int lead = (int)((t_own - tin[0]) << 8) >> 8;
    lead = max(lead, __builtin_amdgcn_mov_dpp(lead, XOR1, 0xf, 0xf, true));
    lead = max(lead, __builtin_amdgcn_mov_dpp(lead, XOR2, 0xf, 0xf, true));
    lead = max(lead, __builtin_amdgcn_mov_dpp(lead, ROR4, 0xf, 0xf, true));
    lead = max(lead, __builtin_amdgcn_mov_dpp(lead, ROR8, 0xf, 0xf, true));
    lead = max(__builtin_amdgcn_readlane(lead, 0), __builtin_amdgcn_readlane(lead, 16));
    if (lane == 0) {
      lds_put(cnt + C_ANCHOR, t_own - (unsigned)min(max(lead, 0), 1000));
      lds_put(cnt + C_DUE0, t_own - (unsigned)min(max(lead, 0), 1000) + kT0 - phase);
    }
    lds_arrive(cnt + C_QIMG, lane);
  }
  PIN();
  LOAD_K(0, 0);
  LOAD_V(0, 0);
  LOAD_K(1, 0);
  LOAD_V(1, 0);
  if (!streamer || lds_get(cnt + C_QIMG) == 0u) SETTLE();
  lds_wait<1>(cnt + C_QIMG, 1u);
  const v4u tagv = *reinterpret_cast<const v4u*>(lds + L_TAG);
  const int pp = (lane >> 3) & 1, hh = lane & 7, gg = lane >> 4;
  const v4u Q0 = *reinterpret_cast<const v4u*>(lds + L_QIMG + hh * 256 + pp * 64 + gg * 16);
  const v4u Q1 = *reinterpret_cast<const v4u*>(lds + L_QIMG + hh * 256 + 128 + pp * 64 + gg * 16);

  f32x4 acc[8];
#pragma unroll
  for (int t = 0; t < 8; ++t) acc[t] = f32x4{0.f, 0.f, 0.f, 0.f};
  float m = -INFINITY, l = 0.f;
  const unsigned pmask = pp ? 0xFFFF0000u : 0x0000FFFFu;
#define TILE(buf, AFTER_QK, AFTER_PV)                                                                                \
  do {                                                                                                               \
    float s[4];                                                                                                      \
    _Pragma("unroll") for (int T = 0; T < 4; ++T) {                                                                  \
      f32x4 d = mfma16(Kt[buf][2 * T], Q0, f32x4{0.f, 0.f, 0.f, 0.f});                                               \
      d = mfma16(Kt[buf][2 * T + 1], Q1, d);                                                                         \
      s[T] = (pp ? d[3] : d[0]) + dpp<ROR8>(pp ? d[1] : d[2]);                                                       \
    }                                                                                                                \
    PIN();                                                                                                           \
    AFTER_QK;                                                                                                        \
    float mt = fmaxf(fmaxf(s[0], s[1]), fmaxf(s[2], s[3]));                                                          \
    mt = fmaxf(mt, dpp<ROR8>(mt));                                                                                   \
    mt = swap32_max(swap16_max(mt)) * kC2;                                                                           \
    const float mn = fmaxf(m, mt);                                                                                   \
    if (__any(mn != m)) {                                                                                            \
      const float alpha = __builtin_amdgcn_exp2f(m - mn);                                                            \
      l *= alpha;                                                                                                    \
      _Pragma("unroll") for (int i = 0; i < 8; ++i) acc[i] *= alpha;                                                 \
      m = mn;                                                                                                        \
    }                                                                                                                \
    v4u P;                                                                                                           \
    _Pragma("unroll") for (int T = 0; T < 4; ++T) {                                                                  \
      const float e = __builtin_amdgcn_exp2f(fmaf(s[T], kC2, -m));                                                   \
      l += e;                                                                                                        \
      P[T] = pack_bf16(e, e) & pmask;                                                                                \
    }                                                                                                                \
    _Pragma("unroll") for (int i = 0; i < 8; ++i) acc[i] = mfma16(column_of(Vt[buf], i), P, acc[i]);                 \
    PIN();                                                                                                           \
    AFTER_PV;                                                                                                        \
  } while (0)
  const int nunit = streamer ? kUnitsS : 1;
#pragma unroll 1
  for (int u = 1; u < nunit; ++u) {
    TILE(0, LOAD_K(0, u), LOAD_V(0, u));
    TILE(1, LOAD_K(1, u), LOAD_V(1, u));
  }
  v4u rdy = ld16<AUX_SC1>(rws, wave < 2 ? lane * 16 : kDrop, WS_RDY + br * 1024);
  PIN();
  if (streamer) lds_arrive(cnt + C_LAST, lane);
  PIN();
  TILE(0, PIN(), PIN());
  TILE(1, PIN(), PIN());
#undef TILE
#undef LOAD_K
#undef LOAD_V
#undef GATE

  l += dpp<ROR8>(l);
  l = swap16_add(l, l);
  l = swap32_add(l, l);
  {
    float* dst = reinterpret_cast<float*>(lds + L_MRG) + (wave * 8 + hh) * 132 + 32 * gg + 16 * pp;
#pragma unroll
    for (int rr = 0; rr < 2; ++rr)
#pragma unroll
      for (int q = 0; q < 2; ++q) {
        f32x4 o;
#pragma unroll
        for (int e = 0; e < 4; ++e) {
          const f32x4 a = acc[4 * q + e];
          o[e] = (pp ? a[2 + rr] : a[rr]) + dpp<ROR8>(pp ? a[rr] : a[2 + rr]);
        }
        *reinterpret_cast<f32x4*>(dst + 8 * rr + 4 * q) = o;
      }
    if (lane < 8) {
      float* ml = reinterpret_cast<float*>(lds + L_MRGML) + lane * 16;
      ml[wave] = m;
      ml[8 + wave] = l;
    }
  }
  SETTLE();
  lds_arrive(cnt + C_PART, lane);
  if (streamer) {
    lds_wait(cnt + C_PART, 8u);
    {
      const int hd = 2 * wave + (lane >> 5);
      const float* ml = reinterpret_cast<const float*>(lds + L_MRGML) + hd * 16;
      const f32x4 m0 = *reinterpret_cast<const f32x4*>(ml), m1 = *reinterpret_cast<const f32x4*>(ml + 4);
      const f32x4 l0 = *reinterpret_cast<const f32x4*>(ml + 8), l1 = *reinterpret_cast<const f32x4*>(ml + 12);
      f32x4 o[8];
#pragma unroll
      for (int w = 0; w < 8; ++w) o[w] = *reinterpret_cast<const f32x4*>(lds + L_MRG + ((w * 8 + hd) * 132 + 4 * (lane & 31)) * 4);
      const float M = fmaxf(fmaxf(fmaxf(m0[0], m0[1]), fmaxf(m0[2], m0[3])), fmaxf(fmaxf(m1[0], m1[1]), fmaxf(m1[2], m1[3])));
      float L = 0.f;
      f32x4 os = {0.f, 0.f, 0.f, 0.f};
#pragma unroll
      for (int w = 0; w < 8; ++w) {
        const float e = __builtin_amdgcn_exp2f((w < 4 ? m0[w & 3] : m1[w & 3]) - M);
        L = fmaf(e, w < 4 ? l0[w & 3] : l1[w & 3], L);
        os += e * o[w];
      }
      const float inv = __builtin_amdgcn_rcpf(L);
      const unsigned u0 = quiet(os[0] * inv), u1 = quiet(os[1] * inv), u2 = quiet(os[2] * inv), u3 = quiet(os[3] * inv);
      const v4u ov = {quiet(M + __builtin_amdgcn_logf(L)), __builtin_amdgcn_perm(u0, u1, 0x07060501u),
                      __builtin_amdgcn_perm(u2, u1, 0x07060302u), __builtin_amdgcn_perm(u3, u2, 0x07060501u)};
      __builtin_amdgcn_raw_buffer_store_b128(ov, rws, WS_PART + wg * kPartSlot + wave * 1024 + lane * 16, 0, AUX_SC1);
    }
    PIN();
    if (wave < 2) {
#define RDY_BAD() __any(((rdy[0] ^ tagv[2]) | (rdy[1] ^ tagv[3]) | (rdy[2] ^ tagv[2]) | (rdy[3] ^ tagv[3])) != 0u)
      if (RDY_BAD()) {
        unsigned spins = 0;
        do {
          nap(kNapPoll);
          rdy = ld16<AUX_SC1>(rws, lane * 16, WS_RDY + br * 1024);
        } while (RDY_BAD() && ++spins < kSpinCap);
      }
#undef RDY_BAD
      const int po = WS_PART + (pair * 32 + wave * 16 + (lane >> 3)) * kPartSlot + idx * 128 + 16 * (lane & 7);
      nap(kNap1);
      v4u pa = ld16<AUX_SC1>(rws, po, 0), pb = ld16<AUX_SC1>(rws, po, 8 * kPartSlot);
      for (unsigned spins = 0;; ++spins) {
        const bool ok = max(umax4(pa), umax4(pb)) != kSent;
        if (__all(ok) || spins > kSpinCap) break;
        nap(kNapPoll);
        if (!ok) {
          pa = ld16<AUX_SC1>(rws, po, 0);
          pb = ld16<AUX_SC1>(rws, po, 8 * kPartSlot);
        }
      }
      const float la = __uint_as_float(pa[0]), lb = __uint_as_float(pb[0]);
      float M = fmaxf(la, lb);
      M = fmaxf(M, dpp<ROR8>(M));
      M = swap32_max(swap16_max(M));
      const float ea = __builtin_amdgcn_exp2f(la - M), eb = __builtin_amdgcn_exp2f(lb - M);
      float v[5] = {ea + eb,
                    ea * __uint_as_float(pa[1] & 0xFFFFFF00u) + eb * __uint_as_float(pb[1] & 0xFFFFFF00u),
                    ea * __uint_as_float(__builtin_amdgcn_perm(pa[2], pa[1], 0x0504000Cu)) +
                        eb * __uint_as_float(__builtin_amdgcn_perm(pb[2], pb[1], 0x0504000Cu)),
                    ea * __uint_as_float(__builtin_amdgcn_perm(pa[2], pa[3], 0x0706000Cu)) +
                        eb * __uint_as_float(__builtin_amdgcn_perm(pb[2], pb[3], 0x0706000Cu)),
                    ea * __uint_as_float(pa[3] & 0xFFFFFF00u) + eb * __uint_as_float(pb[3] & 0xFFFFFF00u)};
#pragma unroll
      for (int i = 0; i < 5; ++i) v[i] += dpp<ROR8>(v[i]);
#pragma unroll
      for (int i = 0; i < 5; ++i) v[i] = swap16_add(v[i], v[i]);
#pragma unroll
      for (int i = 0; i < 5; ++i) v[i] = swap32_add(v[i], v[i]);
      const float inv = __builtin_amdgcn_rcpf(v[0]);
      const v2u ob = {pack_bf16(v[1] * inv, v[2] * inv), pack_bf16(v[3] * inv, v[4] * inv)};
      if (lane < 8) *reinterpret_cast<v2u*>(lds + L_OIMG + wave * 64 + lane * 8) = ob;
    }
    wg_barrier();
    if (wave != 0) return;
  } else {
    if (wave == 6) {
      const unsigned anchor = uni(lds_get(cnt + C_ANCHOR)), t8 = uni(tagv[2]), th = uni(tagv[3]);
      const unsigned t_give = anchor + kT0 + ((kMark2 * kTau16) >> 4) + kMarkWait;
      while (lds_get(cnt + C_MARK2) == 0u && (int)(now_ticks() - t_give) < 0) __builtin_amdgcn_s_sleep(4);
      const unsigned m1 = lds_get(cnt + C_MARK1), m2r = lds_get(cnt + C_MARK2), m2 = m2r ? m2r : t_give;
      const unsigned span = m1 ? min(m2 - m1, 4095u) : 4095u;
      __builtin_amdgcn_raw_buffer_store_b64(v2u{t8 | (span << 12) | min(m2 - anchor, 4095u), th}, rws,
                                            lane == 0 ? WS_LAG + wg * 8 : kDrop, 0, AUX_SC1);
      wait_until(t_give + 120u);
      v2u lg;
      bool bad;
      int tries = 0;
      do {
        lg = __builtin_amdgcn_raw_buffer_load_b64(rws, WS_LAG + pair * 256 + (lane & 31) * 8, 0, AUX_SC1);
        bad = __any((((lg[0] ^ t8) >> 24) | (lg[1] ^ th)) != 0u);
        if (bad) nap(60);
      } while (bad && ++tries < 4);
      if (!bad) {
        float sp = (float)((lg[0] >> 12) & 4095u), at = (float)(lg[0] & 4095u);
        sp += dpp<XOR1>(sp);
        at += dpp<XOR1>(at);
        sp += dpp<XOR2>(sp);
        at += dpp<XOR2>(at);
        sp += dpp<ROR4>(sp);
        at += dpp<ROR4>(at);
        sp += dpp<ROR8>(sp);
        at += dpp<ROR8>(at);
        const float sl = swap16_add(sp, sp) * (1.f / (32.f * (float)(kMark2 - kMark1)));
        at = swap16_add(at, at) * (1.f / 32.f);
        if (sl > kSlowRatio * (float)kTau16 / 16.f || at > (float)(kT0 + ((kMark2 * kTau16) >> 4)) + kSlowLag) {
          const float s2 = fminf(fmaxf(sl * kSlowMargin, kSlowMin), kSlowMax);
          if (lane == 0) {
            lds_put(cnt + C_DUE0, anchor + (unsigned)(int)(at - (float)kMark2 * s2 - kSlowBack) + 16u - phase);
            lds_put(cnt + C_SLOPE, (unsigned)(s2 * 64.f));
          }
        }
      }
    }
    lds_wait<2>(cnt + C_LAST, 4u);
    const unsigned t_wo = now_ticks();
    PIN();
    v4u wor[16], wc[16];
#pragma unroll
    for (int i = 0; i < 16; ++i) {
      if ((i & 7) == 0) {
        unsigned tk = 0;
        if (lane == 0) tk = __hip_atomic_fetch_add(cnt + C_TICKET, 1u, __ATOMIC_RELAXED, __HIP_MEMORY_SCOPE_WORKGROUP);
        tk = uni(tk);
        const unsigned d0 = lds_get(cnt + C_DUE0), sl = uni(lds_get(cnt + C_SLOPE));
        wait_until(uni(d0) + ((tk * sl) >> 6));
        wait_until(t_wo + (((max(tk, 128u) - 128u) * sl) >> 6));
      }
      wor[i] = ld16<AUX_NT>(ro, gg * 8 * 2048 + (lane & 15) * 16, (i & 7) * 2048 + (i >> 3) * 256 + (wave & 3) * 512);
    }
    PIN();
#pragma unroll
    for (int e = 0; e < 16; ++e) {
      wc[e] = column_of(wor + (e & 8), e & 7);
      asm volatile("" : "+v"(wc[e]));
    }
    int ba[8];
#pragma unroll
    for (int e = 0; e < 8; ++e) {
      ba[e] = ((lane & 15) >> 1) == e ? L_OIMG + (lane & 1) * 64 + gg * 16 : L_ZERO;
      asm volatile("" : "+v"(ba[e]));
    }
    wg_barrier();
    v4u bf[8];
#pragma unroll
    for (int e = 0; e < 8; ++e) bf[e] = *reinterpret_cast<const v4u*>(lds + ba[e]);
    f32x4 d0 = {0.f, 0.f, 0.f, 0.f}, d1 = {0.f, 0.f, 0.f, 0.f};
#pragma unroll
    for (int e = 0; e < 8; ++e) {
      d0 = mfma16(wc[e], bf[e], d0);
      d1 = mfma16(wc[8 + e], bf[e], d1);
    }
    const v4u o0 = {not_sentinel(d0[0]), not_sentinel(d0[1]), not_sentinel(d0[2]), not_sentinel(d0[3])};
    const v4u o1 = {not_sentinel(d1[0]), not_sentinel(d1[1]), not_sentinel(d1[2]), not_sentinel(d1[3])};
    const int so = WS_OP + wg * 8192 + (wave & 3) * 2048 + lane * 16;
    __builtin_amdgcn_raw_buffer_store_b128(o0, rws, so, 0, AUX_SC1);
    __builtin_amdgcn_raw_buffer_store_b128(o1, rws, so + 1024, 0, AUX_SC1);
    return;
  }
#undef SETTLE
#undef REPORT

  const int r = wg & 127;
  const int fo = WS_OP + (br * 128 + (lane >> 2)) * 8192 + r * 64 + (lane & 3) * 16;
  const int pos = 4 * r + (lane & 3), ln = pos & 63;
  unsigned short* dst = p.out + (ln & 1) * 2048 + br * 1024 + 128 * (pos >> 6) + 32 * (ln >> 4) + 8 * (lane >> 4) + ((ln & 15) >> 1);
  asm volatile("" : "+v"(dst));
  v4u f[8];
  nap(kNap2);
#pragma unroll
  for (int q = 0; q < 8; ++q) f[q] = ld16<AUX_SC1>(rws, fo, q * 16 * 8192);
  for (unsigned spins = 0;; ++spins) {
    unsigned mx = 0u;
#pragma unroll
    for (int q = 0; q < 8; ++q) mx = max(mx, umax4(f[q]));
    const bool ok = mx != kSent;
    if (__all(ok) || spins > kSpinCap) break;
    nap(kNapPoll);
    if (!ok) {
#pragma unroll
      for (int q = 0; q < 8; ++q) f[q] = ld16<AUX_SC1>(rws, fo, q * 16 * 8192);
    }
  }
  float y[4];
#pragma unroll
  for (int e = 0; e < 4; ++e)
    y[e] = ((__uint_as_float(f[0][e]) + __uint_as_float(f[1][e])) + (__uint_as_float(f[2][e]) + __uint_as_float(f[3][e]))) +
           ((__uint_as_float(f[4][e]) + __uint_as_float(f[5][e])) + (__uint_as_float(f[6][e]) + __uint_as_float(f[7][e])));
  const float z0 = swap32_add(y[0], y[2]), z1 = swap32_add(y[1], y[3]);
  float z = swap16_add(z0, z1);
  z += dpp<ROR4>(z);
  z += dpp<ROR8>(z);
  if ((lane & 12) == 0) *(__attribute__((address_space(1))) unsigned short*)dst = (unsigned short)(pack_bf16(z, z) & 0xFFFFu);
  if (r == 0 && lane == 0) {
    const u64 nx = (((u64)tagv[1] << 32) | tagv[0]) + 1;
    __builtin_amdgcn_raw_buffer_store_b64(v2u{(unsigned)nx, (unsigned)(nx >> 32)}, rws, WS_EPOCH + br * 256, 0, AUX_SC1);
  }
}

}

extern "C" void kernel_launch(void* const* d_in, const int* in_sizes, int n_in, void* d_out, int out_size, void* d_ws,
                              size_t ws_size, hipStream_t stream) {
  (void)in_sizes; (void)n_in; (void)out_size; (void)ws_size;
  Params p;
  for (int i = 0; i < 2; ++i) {
    p.x[i] = (const unsigned short*)d_in[5 * i + 0];
    p.k[i] = (const unsigned short*)d_in[5 * i + 1];
    p.v[i] = (const unsigned short*)d_in[5 * i + 2];
    p.wq[i] = (const unsigned short*)d_in[5 * i + 3];
    p.wo[i] = (const unsigned short*)d_in[5 * i + 4];
  }
  p.out = (unsigned short*)d_out;
  p.ws = (unsigned char*)d_ws;
  hipLaunchKernelGGL(fused_decode, dim3(kNWG), dim3(kNThr), 0, stream, p);
}
```

```cpp
#include <hip/hip_runtime.h>
#include <hip/hip_bf16.h>
#include <stdint.h>

typedef unsigned v4u __attribute__((ext_vector_type(4)));
typedef unsigned v2u __attribute__((ext_vector_type(2)));
typedef float f32x4 __attribute__((ext_vector_type(4)));
typedef __bf16 bf16x8 __attribute__((ext_vector_type(8)));
typedef __bf16 bf16x2 __attribute__((ext_vector_type(2)));
typedef unsigned long long u64;
typedef __amdgpu_buffer_rsrc_t rsrc_t;

namespace {

constexpr int kD = 1024, kHkv = 4, kSk = 32768, kDh = 128;
constexpr int kNWG = 256, kNThr = 512;
constexpr int kUnitsS = 7;
constexpr int AUX_NT = 2, AUX_SC1 = 16;
constexpr int RSRC_W3 = 0x00020000;
constexpr unsigned kSent = 0xFFFFFFFFu;
constexpr int kDrop = 0x7FFFFF00;
constexpr unsigned kSpinCap = 1u << 18;
constexpr float kC2 = 0.088388347648318440f * 1.4426950408889634f;

#define SCHED_TAU 450
constexpr unsigned kT0 = 400;
constexpr unsigned kTau16 = SCHED_TAU;
constexpr int kWin16 = 52, kWinGain = 35;
constexpr int kLate = 100;
constexpr unsigned kMark1 = 44, kMark2 = 68;
constexpr unsigned kMarkWait = 250;
constexpr unsigned kMarkAt = kT0 + ((kMark2 * kTau16) >> 4), kMarkSpan = ((kMark2 - kMark1) * kTau16) >> 4;
constexpr unsigned kQuietLag = 150, kUsualLag = 90;
constexpr unsigned kTGather = 540;
constexpr unsigned kNap1 = 70, kNap2 = 95, kNapPoll = 20;

constexpr int WS_EPOCH = 0;
constexpr int WS_RDY = 2048;
constexpr int WS_TIN = 4096;
constexpr int WS_LAG = 6144;
constexpr int WS_QG = 8192;
constexpr int kPartSlot = 4096;
constexpr int WS_PART = WS_QG + 8 * 2 * 2 * 1024 * 8;
constexpr int WS_OP = WS_PART + 256 * kPartSlot;
constexpr int WS_END = WS_OP + 256 * 8192;

constexpr int L_X = 0;
constexpr int L_TAG = L_X + 2048;
constexpr int L_CNT = L_TAG + 16;
constexpr int L_QRED = L_CNT + 144;
constexpr int L_QIMG = L_QRED + 4 * 512;
constexpr int L_OIMG = L_QIMG + 2048;
constexpr int L_ZERO = L_OIMG + 128;
constexpr int L_MRGML = L_ZERO + 16;
constexpr int L_MRG = L_MRGML + 8 * 16 * 4;
constexpr int L_END = L_MRG + 8 * 8 * 132 * 4;

enum {
  C_QPART, C_QIMG, C_RESET, C_LAST, C_PART,
  C_TICKET, C_LANDED, C_ANCHOR,
  C_PROG = 8,
  C_DUE0 = 12, C_SLOPE, C_MARK1, C_MARK2,
  C_RING = 16,
  C_RINGPOS = 32,
  C_WORDS
};

struct Params {
  const unsigned short* x[2];
  const unsigned short* k[2];
  const unsigned short* v[2];
  const unsigned short* wq[2];
  const unsigned short* wo[2];
  unsigned short* out;
  unsigned char* ws;
};

__device__ __forceinline__ rsrc_t make_rsrc(const void* p, int bytes) {
  return __builtin_amdgcn_make_buffer_rsrc((void*)p, 0, bytes, RSRC_W3);
}
template <int AUX> __device__ __forceinline__ v4u ld16(rsrc_t r, int voff, int soff) {
  return __builtin_amdgcn_raw_buffer_load_b128(r, voff, soff, AUX);
}
__device__ __forceinline__ f32x4 mfma16(v4u a, v4u b, f32x4 c) {
  return __builtin_amdgcn_mfma_f32_16x16x32_bf16(__builtin_bit_cast(bf16x8, a), __builtin_bit_cast(bf16x8, b), c, 0, 0, 0);
}
__device__ __forceinline__ float dot2(unsigned a, unsigned b, float c) {
  return __builtin_amdgcn_fdot2_f32_bf16(__builtin_bit_cast(bf16x2, a), __builtin_bit_cast(bf16x2, b), c, false);
}
__device__ __forceinline__ unsigned pack_bf16(float lo, float hi) {
  bf16x2 r = {(__bf16)lo, (__bf16)hi};
  return __builtin_bit_cast(unsigned, r);
}
template <int CTRL> __device__ __forceinline__ float dpp(float x) {
  return __builtin_bit_cast(float, __builtin_amdgcn_mov_dpp(__builtin_bit_cast(int, x), CTRL, 0xf, 0xf, true));
}
constexpr int XOR1 = 0xB1, XOR2 = 0x4E, ROR4 = 0x124, ROR8 = 0x128;
__device__ __forceinline__ float swap16_add(float a, float b) {
  auto s = __builtin_amdgcn_permlane16_swap(__float_as_uint(a), __float_as_uint(b), false, false);
  return __uint_as_float(s[0]) + __uint_as_float(s[1]);
}
__device__ __forceinline__ float swap32_add(float a, float b) {
  auto s = __builtin_amdgcn_permlane32_swap(__float_as_uint(a), __float_as_uint(b), false, false);
  return __uint_as_float(s[0]) + __uint_as_float(s[1]);
}
__device__ __forceinline__ float swap16_max(float a) {
  auto s = __builtin_amdgcn_permlane16_swap(__float_as_uint(a), __float_as_uint(a), false, false);
  return fmaxf(__uint_as_float(s[0]), __uint_as_float(s[1]));
}
__device__ __forceinline__ float swap32_max(float a) {
  auto s = __builtin_amdgcn_permlane32_swap(__float_as_uint(a), __float_as_uint(a), false, false);
  return fmaxf(__uint_as_float(s[0]), __uint_as_float(s[1]));
}
__device__ __forceinline__ v4u column_of(const v4u* r, int t) {
  v4u o;
#pragma unroll
  for (int u = 0; u < 4; ++u)
    o[u] = __builtin_amdgcn_perm(r[2 * u + 1][t >> 1], r[2 * u][t >> 1], (t & 1) ? 0x07060302u : 0x05040100u);
  return o;
}
__device__ __forceinline__ unsigned not_sentinel(float f) { return min(__float_as_uint(f), kSent - 1u); }
__device__ __forceinline__ unsigned quiet(float f) { return f != f ? 0x7FC00000u : __float_as_uint(f); }
__device__ __forceinline__ unsigned umax4(v4u a) { return max(max(a[0], a[1]), max(a[2], a[3])); }
__device__ __forceinline__ u64 mix64(u64 z) {
  z += 0x9E3779B97F4A7C15ull;
  z = (z ^ (z >> 30)) * 0xBF58476D1CE4E5B9ull;
  z = (z ^ (z >> 27)) * 0x94D049BB133111EBull;
  return z ^ (z >> 31);
}
#define PIN() __builtin_amdgcn_sched_barrier(0)
#define MEMFENCE() asm volatile("" ::: "memory")
__device__ __forceinline__ void wg_barrier() {
  asm volatile("s_waitcnt lgkmcnt(0)" ::: "memory");
  __builtin_amdgcn_s_barrier();
  MEMFENCE();
}
__device__ __forceinline__ void lds_arrive(unsigned* c, int lane) {
  asm volatile("s_waitcnt lgkmcnt(0)" ::: "memory");
  if (lane == 0) __hip_atomic_fetch_add(c, 1u, __ATOMIC_RELAXED, __HIP_MEMORY_SCOPE_WORKGROUP);
}
template <int SLEEP = 0> __device__ __forceinline__ void lds_wait(unsigned* c, unsigned n) {
  while (__hip_atomic_load(c, __ATOMIC_RELAXED, __HIP_MEMORY_SCOPE_WORKGROUP) < n) __builtin_amdgcn_s_sleep(SLEEP);
  MEMFENCE();
}
__device__ __forceinline__ unsigned now_ticks() { return (unsigned)__builtin_amdgcn_s_memrealtime(); }
__device__ __forceinline__ void wait_until(unsigned t) {
  while ((int)(now_ticks() - t) < 0) __builtin_amdgcn_s_sleep(1);
  MEMFENCE();
}
__device__ __forceinline__ void nap(unsigned ticks) { wait_until(now_ticks() + ticks); }
__device__ __forceinline__ unsigned lds_get(unsigned* c) {
  return __hip_atomic_load(c, __ATOMIC_RELAXED, __HIP_MEMORY_SCOPE_WORKGROUP);
}
__device__ __forceinline__ void lds_put(unsigned* c, unsigned v) {
  __hip_atomic_store(c, v, __ATOMIC_RELAXED, __HIP_MEMORY_SCOPE_WORKGROUP);
}
__device__ __forceinline__ unsigned uni(unsigned v) { return __builtin_amdgcn_readfirstlane(v); }

__global__ void __launch_bounds__(kNThr, 2) fused_decode(Params p) {
  __shared__ __attribute__((aligned(16))) unsigned char lds[L_END];
  const int tid = threadIdx.x, lane = tid & 63;
  const int wave = __builtin_amdgcn_readfirstlane(tid >> 6);
  const bool streamer = wave < 4;
  const int wg = blockIdx.x, pair = wg >> 5, idx = wg & 31;
  const int br = pair >> 2, kvh = pair & 3, b = idx >> 4, split = idx & 15;
  const int qblk = idx & 15, kh = idx >> 4;
  const unsigned t_in = now_ticks();
  PIN();
  const rsrc_t rws = make_rsrc(p.ws, WS_END);
  const rsrc_t ro = make_rsrc(p.wo[br] + (size_t)(kvh * 1024 + idx * 32) * kD, 32 * kD * 2);
  const size_t kv_off = ((size_t)(b * kHkv + kvh) * kSk + (size_t)split * 2048) * kDh;
  const rsrc_t rk = make_rsrc(p.k[br] + kv_off, 2048 * kDh * 2);
  const rsrc_t rv = make_rsrc(p.v[br] + kv_off, 2048 * kDh * 2);
  const int toff0 = ((wave + wg + (wg >> 3) - (streamer ? 0 : 4)) & 7) * 8192, toff1 = toff0 ^ 32768;
  const int blk0 = (wg >> 3) + (wg >> 6) * 3 + (streamer ? 0 : 7);
  const unsigned phase = (wg * 37) & 31;

  unsigned* const cnt = reinterpret_cast<unsigned*>(lds + L_CNT);
  v4u wqr[16], xv;
  v2u ep;
  if (!streamer) {
    const int xw = wave - 4;
    const rsrc_t rq = make_rsrc(p.wq[br] + (size_t)(kh * 512 + xw * 128) * 4096 + kvh * 1024 + qblk * 64, 127 * 8192 + 128);
    const rsrc_t rx = make_rsrc(p.x[br] + kh * 512 + xw * 128, 2048 + 256);
    const int qo = (lane >> 3) * 16 * 8192 + (lane & 7) * 16;
    ep = __builtin_amdgcn_raw_buffer_load_b64(rws, wave == 7 ? WS_EPOCH + br * 256 : kDrop, 0, AUX_SC1);
    xv = ld16<0>(rx, lane < 32 ? (lane >> 4) * 2048 + (lane & 15) * 16 : kDrop, 0);
#pragma unroll
    for (int i = 0; i < 16; ++i) wqr[i] = ld16<AUX_NT>(rq, qo, i * 8192);
    if (tid < 256 + C_WORDS) cnt[lane] = lane == C_ANCHOR ? t_in : lane == C_DUE0 ? t_in + kT0 : lane == C_SLOPE ? 4u * kTau16 : 0u;
    if (tid == 320) *reinterpret_cast<v4u*>(lds + L_ZERO) = v4u{0u, 0u, 0u, 0u};
  }
  PIN();
  wg_barrier();
  PIN();
  const int kl = (lane & 15) >> 1;
  int koff = (4 * (kl & 1) + (kl >> 1)) * 256 + (4 * (lane & 1) + (lane >> 4)) * 16;
  int voff = lane * 16;
  v4u Kt[2][8], Vt[2][8];
  bool owes = false;
  unsigned ngrp = 0;
#define REPORT()                                                                                                     \
  do {                                                                                                               \
    if (lane == 0) {                                                                                                 \
      const unsigned i_ = __hip_atomic_fetch_add(cnt + C_RINGPOS, 1u, __ATOMIC_RELAXED, __HIP_MEMORY_SCOPE_WORKGROUP); \
      lds_put(cnt + C_RING + (i_ & 15u), now_ticks());                                                               \
    }                                                                                                                \
    lds_arrive(cnt + C_LANDED, lane);                                                                                \
    owes = false;                                                                                                    \
  } while (0)
#define GATE()                                                                                                       \
  do {                                                                                                               \
    if (streamer) {                                                                                                  \
      for (int w_ = 0; w_ < 200; ++w_) {                                                                             \
        const unsigned a_ = min(min(lds_get(cnt + C_PROG), lds_get(cnt + C_PROG + 1)),                               \
                                min(lds_get(cnt + C_PROG + 2), lds_get(cnt + C_PROG + 3)));                          \
        if (ngrp <= uni(a_)) break;                                                                                  \
        __builtin_amdgcn_s_sleep(1);                                                                                 \
      }                                                                                                              \
      ++ngrp;                                                                                                        \
      if (lane == 0) lds_put(cnt + C_PROG + wave, ngrp);                                                             \
    }                                                                                                                \
    unsigned tk_ = 0;                                                                                                \
    if (lane == 0) tk_ = __hip_atomic_fetch_add(cnt + C_TICKET, 1u, __ATOMIC_RELAXED, __HIP_MEMORY_SCOPE_WORKGROUP); \
    tk_ = uni(tk_);                                                                                                  \
    const unsigned d0_ = lds_get(cnt + C_DUE0), sl_ = uni(lds_get(cnt + C_SLOPE));                                   \
    const unsigned due_ = uni(d0_) + ((tk_ * sl_) >> 6);                                                             \
    if (owes) {                                                                                                      \
      if ((int)(now_ticks() - due_) < kLate) asm volatile("s_waitcnt vmcnt(0)" ::: "memory");                        \
      else asm volatile("s_waitcnt vmcnt(4)" ::: "memory");                                                          \
      REPORT();                                                                                                      \
    }                                                                                                                \
    owes = true;                                                                                                     \
    for (;;) {                                                                                                       \
      const unsigned now_ = now_ticks();                                                                             \
      const int late_ = (int)(now_ - due_);                                                                          \
      if (late_ >= 0) {                                                                                              \
        const int c16_ = kWin16 + ((late_ * kWinGain) >> 8);                                                         \
        const int n_ = (c16_ + 15) >> 4, need_ = (int)tk_ - n_;                                                      \
        if (need_ < 0) break;                                                                                        \
        const int landed_ = (int)uni(lds_get(cnt + C_LANDED));                                                       \
        if (landed_ > need_ + 1) break;                                                                              \
        if (landed_ > need_ &&                                                                                       \
            (int)(now_ - uni(lds_get(cnt + C_RING + (need_ & 15)))) >= (int)(((n_ * 16 - c16_) * sl_) >> 10))        \
          break;                                                                                                     \
      }                                                                                                              \
      __builtin_amdgcn_s_sleep(1);                                                                                   \
    }                                                                                                                \
    if ((tk_ == kMark1 || tk_ == kMark2) && lane == 0) lds_put(cnt + (tk_ == kMark1 ? C_MARK1 : C_MARK2), now_ticks() | 1u); \
    MEMFENCE();                                                                                                      \
  } while (0)
#define SETTLE()                                                                                                     \
  do {                                                                                                               \
    if (owes) {                                                                                                      \
      asm volatile("s_waitcnt vmcnt(0)" ::: "memory");                                                               \
      REPORT();                                                                                                      \
    }                                                                                                                \
  } while (0)
#define LOAD_K(buf, u)                                                                                               \
  do {                                                                                                               \
    GATE();                                                                                                          \
    asm volatile("" : "+v"(koff));                                                                                   \
    const int so_ = ((blk0 + (u)) & 7) * 65536 + ((buf) ? toff1 : toff0);                                            \
    _Pragma("unroll") for (int i = 0; i < 8; ++i)                                                                    \
        Kt[buf][i] = ld16<AUX_NT>(rk, koff + (i & 2) * 1024 + (i & 1) * 128, so_ + (i >> 2) * 4096);                 \
    PIN();                                                                                                           \
  } while (0)
#define LOAD_V(buf, u)                                                                                               \
  do {                                                                                                               \
    GATE();                                                                                                          \
    asm volatile("" : "+v"(voff));                                                                                   \
    const int so_ = ((blk0 + (u)) & 7) * 65536 + ((buf) ? toff1 : toff0);                                            \
    _Pragma("unroll") for (int i = 0; i < 8; ++i)                                                                    \
        Vt[buf][i] = ld16<AUX_NT>(rv, voff + (i & 3) * 1024, so_ + (i >> 2) * 4096);                                 \
    PIN();                                                                                                           \
  } while (0)

  if (streamer) {
    const int to = lane == 0 ? 0 : kDrop;
    const unsigned t0 = __builtin_amdgcn_raw_buffer_load_b32(rk, to, wave * 131072, AUX_NT);
    const unsigned t1 = __builtin_amdgcn_raw_buffer_load_b32(rv, to, wave * 131072, AUX_NT);
    const unsigned t2 = __builtin_amdgcn_raw_buffer_load_b32(ro, to, wave * 16384, AUX_NT);
    asm volatile("" ::"v"(t0), "v"(t1), "v"(t2));
    const v4u s4 = {kSent, kSent, kSent, kSent};
    __builtin_amdgcn_raw_buffer_store_b128(s4, rws, WS_OP + wg * 8192 + tid * 16, 0, AUX_SC1);
    __builtin_amdgcn_raw_buffer_store_b128(s4, rws, WS_OP + wg * 8192 + 4096 + tid * 16, 0, AUX_SC1);
    __builtin_amdgcn_raw_buffer_store_b128(s4, rws, WS_PART + wg * kPartSlot + tid * 16, 0, AUX_SC1);
    asm volatile("s_waitcnt vmcnt(0)" ::: "memory");
    lds_arrive(cnt + C_RESET, lane);
  } else {
    const int xw = wave - 4;
    if (lane < 32) *reinterpret_cast<v4u*>(lds + L_X + xw * 512 + lane * 16) = xv;
    asm volatile("s_waitcnt lgkmcnt(0)" ::: "memory");
    float qa[16];
#pragma unroll
    for (int i = 0; i < 16; ++i) qa[i] = 0.f;
#pragma unroll
    for (int h = 0; h < 2; ++h) {
      const v4u x0 = *reinterpret_cast<const v4u*>(lds + L_X + xw * 512 + (lane >> 3) * 32 + h * 16);
      const v4u x1 = *reinterpret_cast<const v4u*>(lds + L_X + xw * 512 + 256 + (lane >> 3) * 32 + h * 16);
#pragma unroll
      for (int e = 0; e < 8; ++e) {
        const v4u c = column_of(wqr + 8 * h, e);
#pragma unroll
        for (int u = 0; u < 4; ++u) {
          qa[e] = dot2(c[u], x0[u], qa[e]);
          qa[8 + e] = dot2(c[u], x1[u], qa[8 + e]);
        }
      }
    }
    float r8[8], r4[4];
#pragma unroll
    for (int e = 0; e < 8; ++e) r8[e] = swap32_add(qa[e], qa[8 + e]);
#pragma unroll
    for (int e = 0; e < 4; ++e) r4[e] = swap16_add(r8[e], r8[4 + e]);
    const bool up = lane & 8;
    const float o0 = (up ? r4[2] : r4[0]) + dpp<ROR8>(up ? r4[0] : r4[2]);
    const float o1 = (up ? r4[3] : r4[1]) + dpp<ROR8>(up ? r4[1] : r4[3]);
    *reinterpret_cast<float2*>(lds + L_QRED + xw * 512 + lane * 8) = make_float2(o0, o1);
    lds_arrive(cnt + C_QPART, lane);
  }
  if (wave == 7) {
    const u64 epoch = ((u64)(unsigned)__builtin_amdgcn_readfirstlane(ep[1]) << 32) | (unsigned)__builtin_amdgcn_readfirstlane(ep[0]);
    const u64 tg = mix64(epoch);
    const unsigned t8 = (unsigned)tg & 0xFF000000u, th = (unsigned)(tg >> 32);
    if (lane == 0) *reinterpret_cast<v4u*>(lds + L_TAG) = v4u{(unsigned)epoch, (unsigned)(epoch >> 32), t8, th};
    lds_wait(cnt + C_QPART, 4u);
    float s0 = 0.f, s1 = 0.f;
#pragma unroll
    for (int w = 0; w < 4; ++w) {
      const float2 q = *reinterpret_cast<const float2*>(lds + L_QRED + w * 512 + lane * 8);
      s0 += q.x;
      s1 += q.y;
    }
    const v4u g = {t8 | (__float_as_uint(s0) >> 8), th, t8 | (__float_as_uint(s1) >> 8), th};
    const int col = 8 * (lane & 7) + 4 * ((lane >> 4) & 1) + 2 * ((lane >> 3) & 1);
    __builtin_amdgcn_raw_buffer_store_b128(g, rws, WS_QG + (((pair * 2 + (lane >> 5)) * 2 + kh) * 1024 + qblk * 64 + col) * 8, 0, AUX_SC1);
    const unsigned t_own = uni(lds_get(cnt + C_ANCHOR));
    __builtin_amdgcn_raw_buffer_store_b64(v2u{t8 | (t_own & 0xFFFFFFu), th}, rws, lane == 0 ? WS_TIN + wg * 8 : kDrop, 0, AUX_SC1);
    lds_wait(cnt + C_RESET, 4u);
    __builtin_amdgcn_raw_buffer_store_b64(v2u{t8, th}, rws, lane == 0 ? WS_RDY + wg * 8 : kDrop, 0, AUX_SC1);
    wait_until(t_own + kTGather);
    const int qg_off = WS_QG + (pair * 2 + b) * 16384 + lane * 16;
    v4u qg[16];
    v2u tin;
    unsigned spins = 0, bad;
    do {
      MEMFENCE();
#pragma unroll
      for (int i = 0; i < 16; ++i) qg[i] = ld16<AUX_SC1>(rws, qg_off, i * 1024);
      tin = __builtin_amdgcn_raw_buffer_load_b64(rws, WS_TIN + pair * 256 + (lane & 31) * 8, 0, AUX_SC1);
      unsigned bl = tin[0] ^ t8, bh = tin[1] ^ th;
#pragma unroll
      for (int i = 0; i < 16; ++i) {
        bl |= (qg[i][0] ^ t8) | (qg[i][2] ^ t8);
        bh |= (qg[i][1] ^ th) | (qg[i][3] ^ th);
      }
      bad = (bl >> 24) | bh;
    } while (__any(bad != 0u) && ++spins < kSpinCap);
#pragma unroll
    for (int i = 0; i < 8; ++i)
      *reinterpret_cast<unsigned*>(lds + L_QIMG + i * 256 + lane * 4) =
          pack_bf16(__uint_as_float(qg[i][0] << 8) + __uint_as_float(qg[8 + i][0] << 8),
                    __uint_as_float(qg[i][2] << 8) + __uint_as_float(qg[8 + i][2] << 8));
    int lead = (int)((t_own - tin[0]) << 8) >> 8;
    lead = max(lead, __builtin_amdgcn_mov_dpp(lead, XOR1, 0xf, 0xf, true));
    lead = max(lead, __builtin_amdgcn_mov_dpp(lead, XOR2, 0xf, 0xf, true));
    lead = max(lead, __builtin_amdgcn_mov_dpp(lead, ROR4, 0xf, 0xf, true));
    lead = max(lead, __builtin_amdgcn_mov_dpp(lead, ROR8, 0xf, 0xf, true));
    lead = max(__builtin_amdgcn_readlane(lead, 0), __builtin_amdgcn_readlane(lead, 16));
    if (lane == 0) {
      lds_put(cnt + C_ANCHOR, t_own - (unsigned)min(max(lead, 0), 1000));
      lds_put(cnt + C_DUE0, t_own - (unsigned)min(max(lead, 0), 1000) + kT0 - phase);
    }
    lds_arrive(cnt + C_QIMG, lane);
  }
  PIN();
  LOAD_K(0, 0);
  LOAD_V(0, 0);
  LOAD_K(1, 0);
  LOAD_V(1, 0);
  if (!streamer || lds_get(cnt + C_QIMG) == 0u) SETTLE();
  lds_wait<1>(cnt + C_QIMG, 1u);
  const v4u tagv = *reinterpret_cast<const v4u*>(lds + L_TAG);
  const int pp = (lane >> 3) & 1, hh = lane & 7, gg = lane >> 4;
  const v4u Q0 = *reinterpret_cast<const v4u*>(lds + L_QIMG + hh * 256 + pp * 64 + gg * 16);
  const v4u Q1 = *reinterpret_cast<const v4u*>(lds + L_QIMG + hh * 256 + 128 + pp * 64 + gg * 16);

  f32x4 acc[8];
#pragma unroll
  for (int t = 0; t < 8; ++t) acc[t] = f32x4{0.f, 0.f, 0.f, 0.f};
  float m = -INFINITY, l = 0.f;
  const unsigned pmask = pp ? 0xFFFF0000u : 0x0000FFFFu;
#define TILE(buf, AFTER_QK, AFTER_PV)                                                                                \
  do {                                                                                                               \
    float s[4];                                                                                                      \
    _Pragma("unroll") for (int T = 0; T < 4; ++T) {                                                                  \
      f32x4 d = mfma16(Kt[buf][2 * T], Q0, f32x4{0.f, 0.f, 0.f, 0.f});                                               \
      d = mfma16(Kt[buf][2 * T + 1], Q1, d);                                                                         \
      s[T] = (pp ? d[3] : d[0]) + dpp<ROR8>(pp ? d[1] : d[2]);                                                       \
    }                                                                                                                \
    PIN();                                                                                                           \
    AFTER_QK;                                                                                                        \
    float mt = fmaxf(fmaxf(s[0], s[1]), fmaxf(s[2], s[3]));                                                          \
    mt = fmaxf(mt, dpp<ROR8>(mt));                                                                                   \
    mt = swap32_max(swap16_max(mt)) * kC2;                                                                           \
    const float mn = fmaxf(m, mt);                                                                                   \
    if (__any(mn != m)) {                                                                                            \
      const float alpha = __builtin_amdgcn_exp2f(m - mn);                                                            \
      l *= alpha;                                                                                                    \
      _Pragma("unroll") for (int i = 0; i < 8; ++i) acc[i] *= alpha;                                                 \
      m = mn;                                                                                                        \
    }                                                                                                                \
    v4u P;                                                                                                           \
    _Pragma("unroll") for (int T = 0; T < 4; ++T) {                                                                  \
      const float e = __builtin_amdgcn_exp2f(fmaf(s[T], kC2, -m));                                                   \
      l += e;                                                                                                        \
      P[T] = pack_bf16(e, e) & pmask;                                                                                \
    }                                                                                                                \
    _Pragma("unroll") for (int i = 0; i < 8; ++i) acc[i] = mfma16(column_of(Vt[buf], i), P, acc[i]);                 \
    PIN();                                                                                                           \
    AFTER_PV;                                                                                                        \
  } while (0)
  const int nunit = streamer ? kUnitsS : 1;
#pragma unroll 1
  for (int u = 1; u < nunit; ++u) {
    TILE(0, LOAD_K(0, u), LOAD_V(0, u));
    TILE(1, LOAD_K(1, u), LOAD_V(1, u));
  }
  v4u rdy = ld16<AUX_SC1>(rws, wave < 2 ? lane * 16 : kDrop, WS_RDY + br * 1024);
  PIN();
  if (streamer) lds_arrive(cnt + C_LAST, lane);
  PIN();
  TILE(0, PIN(), PIN());
  TILE(1, PIN(), PIN());
#undef TILE
#undef LOAD_K
#undef LOAD_V
#undef GATE

  l += dpp<ROR8>(l);
  l = swap16_add(l, l);
  l = swap32_add(l, l);
  {
    float* dst = reinterpret_cast<float*>(lds + L_MRG) + (wave * 8 + hh) * 132 + 32 * gg + 16 * pp;
#pragma unroll
    for (int rr = 0; rr < 2; ++rr)
#pragma unroll
      for (int q = 0; q < 2; ++q) {
        f32x4 o;
#pragma unroll
        for (int e = 0; e < 4; ++e) {
          const f32x4 a = acc[4 * q + e];
          o[e] = (pp ? a[2 + rr] : a[rr]) + dpp<ROR8>(pp ? a[rr] : a[2 + rr]);
        }
        *reinterpret_cast<f32x4*>(dst + 8 * rr + 4 * q) = o;
      }
    if (lane < 8) {
      float* ml = reinterpret_cast<float*>(lds + L_MRGML) + lane * 16;
      ml[wave] = m;
      ml[8 + wave] = l;
    }
  }
  SETTLE();
  lds_arrive(cnt + C_PART, lane);
  if (streamer) {
    lds_wait(cnt + C_PART, 8u);
    {
      const int hd = 2 * wave + (lane >> 5);
      const float* ml = reinterpret_cast<const float*>(lds + L_MRGML) + hd * 16;
      const f32x4 m0 = *reinterpret_cast<const f32x4*>(ml), m1 = *reinterpret_cast<const f32x4*>(ml + 4);
      const f32x4 l0 = *reinterpret_cast<const f32x4*>(ml + 8), l1 = *reinterpret_cast<const f32x4*>(ml + 12);
      f32x4 o[8];
#pragma unroll
      for (int w = 0; w < 8; ++w) o[w] = *reinterpret_cast<const f32x4*>(lds + L_MRG + ((w * 8 + hd) * 132 + 4 * (lane & 31)) * 4);
      const float M = fmaxf(fmaxf(fmaxf(m0[0], m0[1]), fmaxf(m0[2], m0[3])), fmaxf(fmaxf(m1[0], m1[1]), fmaxf(m1[2], m1[3])));
      float L = 0.f;
      f32x4 os = {0.f, 0.f, 0.f, 0.f};
#pragma unroll
      for (int w = 0; w < 8; ++w) {
        const float e = __builtin_amdgcn_exp2f((w < 4 ? m0[w & 3] : m1[w & 3]) - M);
        L = fmaf(e, w < 4 ? l0[w & 3] : l1[w & 3], L);
        os += e * o[w];
      }
      const float inv = __builtin_amdgcn_rcpf(L);
      const unsigned u0 = quiet(os[0] * inv), u1 = quiet(os[1] * inv), u2 = quiet(os[2] * inv), u3 = quiet(os[3] * inv);
      const v4u ov = {quiet(M + __builtin_amdgcn_logf(L)), __builtin_amdgcn_perm(u0, u1, 0x07060501u),
                      __builtin_amdgcn_perm(u2, u1, 0x07060302u), __builtin_amdgcn_perm(u3, u2, 0x07060501u)};
      __builtin_amdgcn_raw_buffer_store_b128(ov, rws, WS_PART + wg * kPartSlot + wave * 1024 + lane * 16, 0, AUX_SC1);
    }
    PIN();
    if (wave < 2) {
#define RDY_BAD() __any(((rdy[0] ^ tagv[2]) | (rdy[1] ^ tagv[3]) | (rdy[2] ^ tagv[2]) | (rdy[3] ^ tagv[3])) != 0u)
      if (RDY_BAD()) {
        unsigned spins = 0;
        do {
          nap(kNapPoll);
          rdy = ld16<AUX_SC1>(rws, lane * 16, WS_RDY + br * 1024);
        } while (RDY_BAD() && ++spins < kSpinCap);
      }
#undef RDY_BAD
      const int po = WS_PART + (pair * 32 + wave * 16 + (lane >> 3)) * kPartSlot + idx * 128 + 16 * (lane & 7);
      nap(kNap1);
      v4u pa = ld16<AUX_SC1>(rws, po, 0), pb = ld16<AUX_SC1>(rws, po, 8 * kPartSlot);
      for (unsigned spins = 0;; ++spins) {
        const bool ok = max(umax4(pa), umax4(pb)) != kSent;
        if (__all(ok) || spins > kSpinCap) break;
        nap(kNapPoll);
        if (!ok) {
          pa = ld16<AUX_SC1>(rws, po, 0);
          pb = ld16<AUX_SC1>(rws, po, 8 * kPartSlot);
        }
      }
      const float la = __uint_as_float(pa[0]), lb = __uint_as_float(pb[0]);
      float M = fmaxf(la, lb);
      M = fmaxf(M, dpp<ROR8>(M));
      M = swap32_max(swap16_max(M));
      const float ea = __builtin_amdgcn_exp2f(la - M), eb = __builtin_amdgcn_exp2f(lb - M);
      float v[5] = {ea + eb,
                    ea * __uint_as_float(pa[1] & 0xFFFFFF00u) + eb * __uint_as_float(pb[1] & 0xFFFFFF00u),
                    ea * __uint_as_float(__builtin_amdgcn_perm(pa[2], pa[1], 0x0504000Cu)) +
                        eb * __uint_as_float(__builtin_amdgcn_perm(pb[2], pb[1], 0x0504000Cu)),
                    ea * __uint_as_float(__builtin_amdgcn_perm(pa[2], pa[3], 0x0706000Cu)) +
                        eb * __uint_as_float(__builtin_amdgcn_perm(pb[2], pb[3], 0x0706000Cu)),
                    ea * __uint_as_float(pa[3] & 0xFFFFFF00u) + eb * __uint_as_float(pb[3] & 0xFFFFFF00u)};
#pragma unroll
      for (int i = 0; i < 5; ++i) v[i] += dpp<ROR8>(v[i]);
#pragma unroll
      for (int i = 0; i < 5; ++i) v[i] = swap16_add(v[i], v[i]);
#pragma unroll
      for (int i = 0; i < 5; ++i) v[i] = swap32_add(v[i], v[i]);
      const float inv = __builtin_amdgcn_rcpf(v[0]);
      const v2u ob = {pack_bf16(v[1] * inv, v[2] * inv), pack_bf16(v[3] * inv, v[4] * inv)};
      if (lane < 8) *reinterpret_cast<v2u*>(lds + L_OIMG + wave * 64 + lane * 8) = ob;
    }
    wg_barrier();
    if (wave != 0) return;
  } else {
    if (wave == 6) {
      const unsigned anchor = uni(lds_get(cnt + C_ANCHOR)), t8 = uni(tagv[2]), th = uni(tagv[3]);
      const unsigned t_give = anchor + kMarkAt + kMarkWait;
      while (lds_get(cnt + C_MARK2) == 0u && (int)(now_ticks() - t_give) < 0) __builtin_amdgcn_s_sleep(4);
      const unsigned m1 = lds_get(cnt + C_MARK1), m2r = lds_get(cnt + C_MARK2), m2 = m2r ? m2r : t_give;
      const unsigned span = m1 ? min(m2 - m1, 4095u) : 4095u;
      const bool tell = m2 - anchor > kMarkAt + kQuietLag || span * 100u > kMarkSpan * 103u;
      __builtin_amdgcn_raw_buffer_store_b64(v2u{t8 | (span << 12) | min(m2 - anchor, 4095u), th}, rws,
                                            lane == 0 && tell ? WS_LAG + wg * 8 : kDrop, 0, AUX_SC1);
      wait_until(t_give + 120u);
      const v2u lg = __builtin_amdgcn_raw_buffer_load_b64(rws, WS_LAG + pair * 256 + (lane & 31) * 8, 0, AUX_SC1);
      const bool none = (((lg[0] ^ t8) >> 24) | (lg[1] ^ th)) != 0u;
      float sp = (float)(none ? kMarkSpan : (lg[0] >> 12) & 4095u), at = (float)(none ? kMarkAt + kUsualLag : lg[0] & 4095u);
      sp += dpp<XOR1>(sp);
      at += dpp<XOR1>(at);
      sp += dpp<XOR2>(sp);
      at += dpp<XOR2>(at);
      sp += dpp<ROR4>(sp);
      at += dpp<ROR4>(at);
      sp += dpp<ROR8>(sp);
      at += dpp<ROR8>(at);
      sp = swap16_add(sp, sp) * (1.f / (32.f * (float)(kMark2 - kMark1)));
      at = swap16_add(at, at) * (1.f / 32.f);
      const float sl = fminf(fmaxf(1.005f * sp, ((float)kTau16 + 0.4f * (at - (float)(kMarkAt + 130))) / 16.f), 32.5f);
      if (sl > 1.02f * (float)kTau16 / 16.f && lane == 0) {
        lds_put(cnt + C_DUE0, anchor + (unsigned)(int)(at - (float)kMark2 * sl) - 44u - phase);
        lds_put(cnt + C_SLOPE, (unsigned)(sl * 64.f));
      }
    }
    lds_wait<2>(cnt + C_LAST, 4u);
    const unsigned t_wo = now_ticks();
    PIN();
    v4u wor[16], wc[16];
#pragma unroll
    for (int i = 0; i < 16; ++i) {
      if ((i & 7) == 0) {
        unsigned tk = 0;
        if (lane == 0) tk = __hip_atomic_fetch_add(cnt + C_TICKET, 1u, __ATOMIC_RELAXED, __HIP_MEMORY_SCOPE_WORKGROUP);
        tk = uni(tk);
        const unsigned d0 = lds_get(cnt + C_DUE0), sl = uni(lds_get(cnt + C_SLOPE));
        wait_until(uni(d0) + ((tk * sl) >> 6));
        wait_until(t_wo + (((max(tk, 128u) - 128u) * sl) >> 6));
      }
      wor[i] = ld16<AUX_NT>(ro, gg * 8 * 2048 + (lane & 15) * 16, (i & 7) * 2048 + (i >> 3) * 256 + (wave & 3) * 512);
    }
    PIN();
#pragma unroll
    for (int e = 0; e < 16; ++e) {
      wc[e] = column_of(wor + (e & 8), e & 7);
      asm volatile("" : "+v"(wc[e]));
    }
    int ba[8];
#pragma unroll
    for (int e = 0; e < 8; ++e) {
      ba[e] = ((lane & 15) >> 1) == e ? L_OIMG + (lane & 1) * 64 + gg * 16 : L_ZERO;
      asm volatile("" : "+v"(ba[e]));
    }
    wg_barrier();
    v4u bf[8];
#pragma unroll
    for (int e = 0; e < 8; ++e) bf[e] = *reinterpret_cast<const v4u*>(lds + ba[e]);
    f32x4 d0 = {0.f, 0.f, 0.f, 0.f}, d1 = {0.f, 0.f, 0.f, 0.f};
#pragma unroll
    for (int e = 0; e < 8; ++e) {
      d0 = mfma16(wc[e], bf[e], d0);
      d1 = mfma16(wc[8 + e], bf[e], d1);
    }
    const v4u o0 = {not_sentinel(d0[0]), not_sentinel(d0[1]), not_sentinel(d0[2]), not_sentinel(d0[3])};
    const v4u o1 = {not_sentinel(d1[0]), not_sentinel(d1[1]), not_sentinel(d1[2]), not_sentinel(d1[3])};
    const int so = WS_OP + wg * 8192 + (wave & 3) * 2048 + lane * 16;
    __builtin_amdgcn_raw_buffer_store_b128(o0, rws, so, 0, AUX_SC1);
    __builtin_amdgcn_raw_buffer_store_b128(o1, rws, so + 1024, 0, AUX_SC1);
    return;
  }
#undef SETTLE
#undef REPORT

  const int r = wg & 127;
  const int fo = WS_OP + (br * 128 + (lane >> 2)) * 8192 + r * 64 + (lane & 3) * 16;
  const int pos = 4 * r + (lane & 3), ln = pos & 63;
  unsigned short* dst = p.out + (ln & 1) * 2048 + br * 1024 + 128 * (pos >> 6) + 32 * (ln >> 4) + 8 * (lane >> 4) + ((ln & 15) >> 1);
  asm volatile("" : "+v"(dst));
  v4u f[8];
  nap(kNap2);
#pragma unroll
  for (int q = 0; q < 8; ++q) f[q] = ld16<AUX_SC1>(rws, fo, q * 16 * 8192);
  for (unsigned spins = 0;; ++spins) {
    unsigned mx = 0u;
#pragma unroll
    for (int q = 0; q < 8; ++q) mx = max(mx, umax4(f[q]));
    const bool ok = mx != kSent;
    if (__all(ok) || spins > kSpinCap) break;
    nap(kNapPoll);
    if (!ok) {
#pragma unroll
      for (int q = 0; q < 8; ++q) f[q] = ld16<AUX_SC1>(rws, fo, q * 16 * 8192);
    }
  }
  float y[4];
#pragma unroll
  for (int e = 0; e < 4; ++e)
    y[e] = ((__uint_as_float(f[0][e]) + __uint_as_float(f[1][e])) + (__uint_as_float(f[2][e]) + __uint_as_float(f[3][e]))) +
           ((__uint_as_float(f[4][e]) + __uint_as_float(f[5][e])) + (__uint_as_float(f[6][e]) + __uint_as_float(f[7][e])));
  const float z0 = swap32_add(y[0], y[2]), z1 = swap32_add(y[1], y[3]);
  float z = swap16_add(z0, z1);
  z += dpp<ROR4>(z);
  z += dpp<ROR8>(z);
  if ((lane & 12) == 0) *(__attribute__((address_space(1))) unsigned short*)dst = (unsigned short)(pack_bf16(z, z) & 0xFFFFu);
  if (r == 0 && lane == 0) {
    const u64 nx = (((u64)tagv[1] << 32) | tagv[0]) + 1;
    __builtin_amdgcn_raw_buffer_store_b64(v2u{(unsigned)nx, (unsigned)(nx >> 32)}, rws, WS_EPOCH + br * 256, 0, AUX_SC1);
  }
}

}

extern "C" void kernel_launch(void* const* d_in, const int* in_sizes, int n_in, void* d_out, int out_size, void* d_ws,
                              size_t ws_size, hipStream_t stream) {
  (void)in_sizes; (void)n_in; (void)out_size; (void)ws_size;
  Params p;
  for (int i = 0; i < 2; ++i) {
    p.x[i] = (const unsigned short*)d_in[5 * i + 0];
    p.k[i] = (const unsigned short*)d_in[5 * i + 1];
    p.v[i] = (const unsigned short*)d_in[5 * i + 2];
    p.wq[i] = (const unsigned short*)d_in[5 * i + 3];
    p.wo[i] = (const unsigned short*)d_in[5 * i + 4];
  }
  p.out = (unsigned short*)d_out;
  p.ws = (unsigned char*)d_ws;
  hipLaunchKernelGGL(fused_decode, dim3(kNWG), dim3(kNThr), 0, stream, p);
}
```

```cpp
#include <hip/hip_runtime.h>
#include <hip/hip_bf16.h>
#include <stdint.h>

typedef unsigned v4u __attribute__((ext_vector_type(4)));
typedef unsigned v2u __attribute__((ext_vector_type(2)));
typedef float f32x4 __attribute__((ext_vector_type(4)));
typedef __bf16 bf16x8 __attribute__((ext_vector_type(8)));
typedef __bf16 bf16x2 __attribute__((ext_vector_type(2)));
typedef unsigned long long u64;
typedef __amdgpu_buffer_rsrc_t rsrc_t;

namespace {

constexpr int kD = 1024, kHkv = 4, kSk = 32768, kDh = 128;
constexpr int kNWG = 256, kNThr = 512;
constexpr int kUnitsS = 7;
constexpr int AUX_NT = 2, AUX_SC1 = 16;
constexpr int RSRC_W3 = 0x00020000;
constexpr unsigned kSent = 0xFFFFFFFFu;
constexpr int kDrop = 0x7FFFFF00;
constexpr unsigned kSpinCap = 1u << 18;
constexpr float kC2 = 0.088388347648318440f * 1.4426950408889634f;

constexpr unsigned kT0 = 400;
constexpr unsigned kTau16 = 450;
constexpr int kWin16 = 52, kWinGain = 35;
constexpr int kLate = 100;
constexpr unsigned kMark1 = 44, kMark2 = 68;
constexpr unsigned kMarkWait = 250;
constexpr unsigned kMarkAt = kT0 + ((kMark2 * kTau16) >> 4), kMarkSpan = ((kMark2 - kMark1) * kTau16) >> 4;
constexpr unsigned kQuietLag = 150, kUsualLag = 90;
constexpr unsigned kTGather = 540;
constexpr unsigned kNap1 = 70, kNap2 = 95, kNapPoll = 20;

constexpr int WS_EPOCH = 0;
constexpr int WS_RDY = 2048;
constexpr int WS_TIN = 4096;
constexpr int WS_LAG = 6144;
constexpr int WS_QG = 8192;
constexpr int kPartSlot = 4096;
constexpr int WS_PART = WS_QG + 8 * 2 * 2 * 1024 * 8;
constexpr int WS_OP = WS_PART + 256 * kPartSlot;
constexpr int WS_END = WS_OP + 256 * 8192;

constexpr int L_X = 0;
constexpr int L_TAG = L_X + 2048;
constexpr int L_CNT = L_TAG + 16;
constexpr int L_QRED = L_CNT + 144;
constexpr int L_QIMG = L_QRED + 4 * 512;
constexpr int L_OIMG = L_QIMG + 2048;
constexpr int L_ZERO = L_OIMG + 128;
constexpr int L_MRGML = L_ZERO + 16;
constexpr int L_MRG = L_MRGML + 8 * 16 * 4;
constexpr int L_END = L_MRG + 8 * 8 * 132 * 4;
static_assert(L_QRED % 16 == 0 && L_QIMG % 16 == 0 && L_OIMG % 16 == 0 && L_MRGML % 16 == 0 && L_MRG % 16 == 0, "LDS alignment");

enum {
  C_QPART, C_QIMG, C_RESET, C_LAST, C_PART,
  C_TICKET, C_LANDED, C_ANCHOR,
  C_PROG = 8,
  C_DUE0 = 12, C_SLOPE, C_MARK1, C_MARK2,
  C_RING = 16,
  C_RINGPOS = 32,
  C_WORDS
};

struct Params {
  const unsigned short* x[2];
  const unsigned short* k[2];
  const unsigned short* v[2];
  const unsigned short* wq[2];
  const unsigned short* wo[2];
  unsigned short* out;
  unsigned char* ws;
};

__device__ __forceinline__ rsrc_t make_rsrc(const void* p, int bytes) {
  return __builtin_amdgcn_make_buffer_rsrc((void*)p, 0, bytes, RSRC_W3);
}
template <int AUX> __device__ __forceinline__ v4u ld16(rsrc_t r, int voff, int soff) {
  return __builtin_amdgcn_raw_buffer_load_b128(r, voff, soff, AUX);
}
__device__ __forceinline__ f32x4 mfma16(v4u a, v4u b, f32x4 c) {
  return __builtin_amdgcn_mfma_f32_16x16x32_bf16(__builtin_bit_cast(bf16x8, a), __builtin_bit_cast(bf16x8, b), c, 0, 0, 0);
}
__device__ __forceinline__ float dot2(unsigned a, unsigned b, float c) {
  return __builtin_amdgcn_fdot2_f32_bf16(__builtin_bit_cast(bf16x2, a), __builtin_bit_cast(bf16x2, b), c, false);
}
__device__ __forceinline__ unsigned pack_bf16(float lo, float hi) {
  bf16x2 r = {(__bf16)lo, (__bf16)hi};
  return __builtin_bit_cast(unsigned, r);
}
template <int CTRL> __device__ __forceinline__ float dpp(float x) {
  return __builtin_bit_cast(float, __builtin_amdgcn_mov_dpp(__builtin_bit_cast(int, x), CTRL, 0xf, 0xf, true));
}
constexpr int XOR1 = 0xB1, XOR2 = 0x4E, ROR4 = 0x124, ROR8 = 0x128;
__device__ __forceinline__ float swap16_add(float a, float b) {
  auto s = __builtin_amdgcn_permlane16_swap(__float_as_uint(a), __float_as_uint(b), false, false);
  return __uint_as_float(s[0]) + __uint_as_float(s[1]);
}
__device__ __forceinline__ float swap32_add(float a, float b) {
  auto s = __builtin_amdgcn_permlane32_swap(__float_as_uint(a), __float_as_uint(b), false, false);
  return __uint_as_float(s[0]) + __uint_as_float(s[1]);
}
__device__ __forceinline__ float swap16_max(float a) {
  auto s = __builtin_amdgcn_permlane16_swap(__float_as_uint(a), __float_as_uint(a), false, false);
  return fmaxf(__uint_as_float(s[0]), __uint_as_float(s[1]));
}
__device__ __forceinline__ float swap32_max(float a) {
  auto s = __builtin_amdgcn_permlane32_swap(__float_as_uint(a), __float_as_uint(a), false, false);
  return fmaxf(__uint_as_float(s[0]), __uint_as_float(s[1]));
}
__device__ __forceinline__ v4u column_of(const v4u* r, int t) {
  v4u o;
#pragma unroll
  for (int u = 0; u < 4; ++u)
    o[u] = __builtin_amdgcn_perm(r[2 * u + 1][t >> 1], r[2 * u][t >> 1], (t & 1) ? 0x07060302u : 0x05040100u);
  return o;
}
__device__ __forceinline__ unsigned not_sentinel(float f) { return min(__float_as_uint(f), kSent - 1u); }
__device__ __forceinline__ unsigned quiet(float f) { return f != f ? 0x7FC00000u : __float_as_uint(f); }
__device__ __forceinline__ unsigned umax4(v4u a) { return max(max(a[0], a[1]), max(a[2], a[3])); }
__device__ __forceinline__ u64 mix64(u64 z) {
  z += 0x9E3779B97F4A7C15ull;
  z = (z ^ (z >> 30)) * 0xBF58476D1CE4E5B9ull;
  z = (z ^ (z >> 27)) * 0x94D049BB133111EBull;
  return z ^ (z >> 31);
}
#define PIN() __builtin_amdgcn_sched_barrier(0)
#define MEMFENCE() asm volatile("" ::: "memory")
__device__ __forceinline__ void wg_barrier() {
  asm volatile("s_waitcnt lgkmcnt(0)" ::: "memory");
  __builtin_amdgcn_s_barrier();
  MEMFENCE();
}
__device__ __forceinline__ void lds_arrive(unsigned* c, int lane) {
  asm volatile("s_waitcnt lgkmcnt(0)" ::: "memory");
  if (lane == 0) __hip_atomic_fetch_add(c, 1u, __ATOMIC_RELAXED, __HIP_MEMORY_SCOPE_WORKGROUP);
}
template <int SLEEP = 0> __device__ __forceinline__ void lds_wait(unsigned* c, unsigned n) {
  while (__hip_atomic_load(c, __ATOMIC_RELAXED, __HIP_MEMORY_SCOPE_WORKGROUP) < n) __builtin_amdgcn_s_sleep(SLEEP);
  MEMFENCE();
}
__device__ __forceinline__ unsigned now_ticks() { return (unsigned)__builtin_amdgcn_s_memrealtime(); }
__device__ __forceinline__ void wait_until(unsigned t) {
  while ((int)(now_ticks() - t) < 0) __builtin_amdgcn_s_sleep(1);
  MEMFENCE();
}
__device__ __forceinline__ void nap(unsigned ticks) { wait_until(now_ticks() + ticks); }
__device__ __forceinline__ unsigned lds_get(unsigned* c) {
  return __hip_atomic_load(c, __ATOMIC_RELAXED, __HIP_MEMORY_SCOPE_WORKGROUP);
}
__device__ __forceinline__ void lds_put(unsigned* c, unsigned v) {
  __hip_atomic_store(c, v, __ATOMIC_RELAXED, __HIP_MEMORY_SCOPE_WORKGROUP);
}
__device__ __forceinline__ unsigned uni(unsigned v) { return __builtin_amdgcn_readfirstlane(v); }
__device__ __forceinline__ u64 line_get(unsigned* c) {
  return __hip_atomic_load(reinterpret_cast<u64*>(c), __ATOMIC_RELAXED, __HIP_MEMORY_SCOPE_WORKGROUP);
}
__device__ __forceinline__ void line_put(unsigned* c, unsigned due0, unsigned slope) {
  __hip_atomic_store(reinterpret_cast<u64*>(c), ((u64)slope << 32) | due0, __ATOMIC_RELAXED, __HIP_MEMORY_SCOPE_WORKGROUP);
}

__global__ void __launch_bounds__(kNThr, 2) fused_decode(Params p) {
  __shared__ __attribute__((aligned(16))) unsigned char lds[L_END];
  const int tid = threadIdx.x, lane = tid & 63;
  const int wave = __builtin_amdgcn_readfirstlane(tid >> 6);
  const bool streamer = wave < 4;
  const int wg = blockIdx.x, pair = wg >> 5, idx = wg & 31;
  const int br = pair >> 2, kvh = pair & 3, b = idx >> 4, split = idx & 15;
  const int qblk = idx & 15, kh = idx >> 4;
  u64 a0 = (u64)p.x[0], a1 = (u64)p.x[1], a2 = (u64)p.k[0], a3 = (u64)p.k[1], a4 = (u64)p.v[0], a5 = (u64)p.v[1];
  u64 a6 = (u64)p.wq[0], a7 = (u64)p.wq[1], a8 = (u64)p.wo[0], a9 = (u64)p.wo[1], a10 = (u64)p.out, a11 = (u64)p.ws;
  const unsigned t_in = now_ticks();
  asm volatile("" : "+s"(a0), "+s"(a1), "+s"(a2), "+s"(a3), "+s"(a4), "+s"(a5), "+s"(a6), "+s"(a7), "+s"(a8),
               "+s"(a9), "+s"(a10), "+s"(a11));
  const unsigned short* const px = (const unsigned short*)(br ? a1 : a0);
  const unsigned short* const pk = (const unsigned short*)(br ? a3 : a2);
  const unsigned short* const pv = (const unsigned short*)(br ? a5 : a4);
  const unsigned short* const pwq = (const unsigned short*)(br ? a7 : a6);
  const unsigned short* const pwo = (const unsigned short*)(br ? a9 : a8);
  PIN();
  const rsrc_t rws = make_rsrc((const void*)a11, WS_END);
  const rsrc_t ro = make_rsrc(pwo + (size_t)(kvh * 1024 + idx * 32) * kD, 32 * kD * 2);
  const size_t kv_off = ((size_t)(b * kHkv + kvh) * kSk + (size_t)split * 2048) * kDh;
  const rsrc_t rk = make_rsrc(pk + kv_off, 2048 * kDh * 2);
  const rsrc_t rv = make_rsrc(pv + kv_off, 2048 * kDh * 2);
  const int toff0 = ((wave + wg + (wg >> 3) - (streamer ? 0 : 4)) & 7) * 8192, toff1 = toff0 ^ 32768;
  const int blk0 = (wg >> 3) + (wg >> 6) * 3 + (streamer ? 0 : 7);
  const unsigned phase = (wg * 37) & 31;

  unsigned* const cnt = reinterpret_cast<unsigned*>(lds + L_CNT);
  v4u wqr[16], xv;
  v2u ep;
  if (!streamer) {
    const int xw = wave - 4;
    const rsrc_t rq = make_rsrc(pwq + (size_t)(kh * 512 + xw * 128) * 4096 + kvh * 1024 + qblk * 64, 127 * 8192 + 128);
    const rsrc_t rx = make_rsrc(px + kh * 512 + xw * 128, 2048 + 256);
    const int qo = (lane >> 3) * 16 * 8192 + (lane & 7) * 16;
    ep = __builtin_amdgcn_raw_buffer_load_b64(rws, wave == 7 ? WS_EPOCH + br * 256 : kDrop, 0, AUX_SC1);
    xv = ld16<0>(rx, lane < 32 ? (lane >> 4) * 2048 + (lane & 15) * 16 : kDrop, 0);
#pragma unroll
    for (int i = 0; i < 16; ++i) wqr[i] = ld16<AUX_NT>(rq, qo, i * 8192);
    if (tid < 256 + C_WORDS) cnt[lane] = lane == C_ANCHOR ? t_in : lane == C_DUE0 ? t_in + kT0 : lane == C_SLOPE ? 4u * kTau16 : 0u;
    if (tid == 320) *reinterpret_cast<v4u*>(lds + L_ZERO) = v4u{0u, 0u, 0u, 0u};
  }
  PIN();
  wg_barrier();
  PIN();
  const int kl = (lane & 15) >> 1;
  int koff = (4 * (kl & 1) + (kl >> 1)) * 256 + (4 * (lane & 1) + (lane >> 4)) * 16;
  int voff = lane * 16;
  v4u Kt[2][8], Vt[2][8];
  bool owes = false;
  unsigned ngrp = 0;
#define REPORT()                                                                                                     \
  do {                                                                                                               \
    if (lane == 0) {                                                                                                 \
      const unsigned i_ = __hip_atomic_fetch_add(cnt + C_RINGPOS, 1u, __ATOMIC_RELAXED, __HIP_MEMORY_SCOPE_WORKGROUP); \
      lds_put(cnt + C_RING + (i_ & 15u), now_ticks());                                                               \
    }                                                                                                                \
    lds_arrive(cnt + C_LANDED, lane);                                                                                \
    owes = false;                                                                                                    \
  } while (0)
#define GATE()                                                                                                       \
  do {                                                                                                               \
    if (streamer) {                                                                                                  \
      for (int w_ = 0; w_ < 200; ++w_) {                                                                             \
        const unsigned a_ = min(min(lds_get(cnt + C_PROG), lds_get(cnt + C_PROG + 1)),                               \
                                min(lds_get(cnt + C_PROG + 2), lds_get(cnt + C_PROG + 3)));                          \
        if (ngrp <= uni(a_)) break;                                                                                  \
        __builtin_amdgcn_s_sleep(1);                                                                                 \
      }                                                                                                              \
      ++ngrp;                                                                                                        \
      if (lane == 0) lds_put(cnt + C_PROG + wave, ngrp);                                                             \
    }                                                                                                                \
    unsigned tk_ = 0;                                                                                                \
    if (lane == 0) tk_ = __hip_atomic_fetch_add(cnt + C_TICKET, 1u, __ATOMIC_RELAXED, __HIP_MEMORY_SCOPE_WORKGROUP); \
    tk_ = uni(tk_);                                                                                                  \
    const u64 ln_ = line_get(cnt + C_DUE0);                                                                          \
    const unsigned sl_ = uni((unsigned)(ln_ >> 32)), due_ = uni((unsigned)ln_) + ((tk_ * sl_) >> 6);                 \
    if (owes) {                                                                                                      \
      if ((int)(now_ticks() - due_) < kLate) asm volatile("s_waitcnt vmcnt(0)" ::: "memory");                        \
      else asm volatile("s_waitcnt vmcnt(4)" ::: "memory");                                                          \
      REPORT();                                                                                                      \
    }                                                                                                                \
    owes = true;                                                                                                     \
    for (;;) {                                                                                                       \
      const unsigned now_ = now_ticks();                                                                             \
      const int late_ = (int)(now_ - due_);                                                                          \
      if (late_ >= 0) {                                                                                              \
        const int c16_ = kWin16 + ((late_ * kWinGain) >> 8);                                                         \
        const int n_ = (c16_ + 15) >> 4, need_ = (int)tk_ - n_;                                                      \
        if (need_ < 0) break;                                                                                        \
        const int landed_ = (int)uni(lds_get(cnt + C_LANDED));                                                       \
        if (landed_ > need_ + 1) break;                                                                              \
        if (landed_ > need_ &&                                                                                       \
            (int)(now_ - uni(lds_get(cnt + C_RING + (need_ & 15)))) >= (int)(((n_ * 16 - c16_) * sl_) >> 10))        \
          break;                                                                                                     \
      }                                                                                                              \
      __builtin_amdgcn_s_sleep(1);                                                                                   \
    }                                                                                                                \
    if ((tk_ == kMark1 || tk_ == kMark2) && lane == 0) lds_put(cnt + (tk_ == kMark1 ? C_MARK1 : C_MARK2), now_ticks() | 1u); \
    MEMFENCE();                                                                                                      \
  } while (0)
#define SETTLE()                                                                                                     \
  do {                                                                                                               \
    if (owes) {                                                                                                      \
      asm volatile("s_waitcnt vmcnt(0)" ::: "memory");                                                               \
      REPORT();                                                                                                      \
    }                                                                                                                \
  } while (0)
#define LOAD_K(buf, u)                                                                                               \
  do {                                                                                                               \
    GATE();                                                                                                          \
    asm volatile("" : "+v"(koff));                                                                                   \
    const int so_ = ((blk0 + (u)) & 7) * 65536 + ((buf) ? toff1 : toff0);                                            \
    _Pragma("unroll") for (int i = 0; i < 8; ++i)                                                                    \
        Kt[buf][i] = ld16<AUX_NT>(rk, koff + (i & 2) * 1024 + (i & 1) * 128, so_ + (i >> 2) * 4096);                 \
    PIN();                                                                                                           \
  } while (0)
#define LOAD_V(buf, u)                                                                                               \
  do {                                                                                                               \
    GATE();                                                                                                          \
    asm volatile("" : "+v"(voff));                                                                                   \
    const int so_ = ((blk0 + (u)) & 7) * 65536 + ((buf) ? toff1 : toff0);                                            \
    _Pragma("unroll") for (int i = 0; i < 8; ++i)                                                                    \
        Vt[buf][i] = ld16<AUX_NT>(rv, voff + (i & 3) * 1024, so_ + (i >> 2) * 4096);                                 \
    PIN();                                                                                                           \
  } while (0)

  if (streamer) {
    const int to = lane == 0 ? 0 : kDrop;
    const unsigned t0 = __builtin_amdgcn_raw_buffer_load_b32(rk, to, wave * 131072, AUX_NT);
    const unsigned t1 = __builtin_amdgcn_raw_buffer_load_b32(rv, to, wave * 131072, AUX_NT);
    const unsigned t2 = __builtin_amdgcn_raw_buffer_load_b32(ro, to, wave * 16384, AUX_NT);
    asm volatile("" ::"v"(t0), "v"(t1), "v"(t2));
    const v4u s4 = {kSent, kSent, kSent, kSent};
    __builtin_amdgcn_raw_buffer_store_b128(s4, rws, WS_OP + wg * 8192 + tid * 16, 0, AUX_SC1);
    __builtin_amdgcn_raw_buffer_store_b128(s4, rws, WS_OP + wg * 8192 + 4096 + tid * 16, 0, AUX_SC1);
    __builtin_amdgcn_raw_buffer_store_b128(s4, rws, WS_PART + wg * kPartSlot + tid * 16, 0, AUX_SC1);
    asm volatile("s_waitcnt vmcnt(0)" ::: "memory");
    lds_arrive(cnt + C_RESET, lane);
  } else {
    const int xw = wave - 4;
    if (lane < 32) *reinterpret_cast<v4u*>(lds + L_X + xw * 512 + lane * 16) = xv;
    asm volatile("s_waitcnt lgkmcnt(0)" ::: "memory");
    float qa[16];
#pragma unroll
    for (int i = 0; i < 16; ++i) qa[i] = 0.f;
#pragma unroll
    for (int h = 0; h < 2; ++h) {
      const v4u x0 = *reinterpret_cast<const v4u*>(lds + L_X + xw * 512 + (lane >> 3) * 32 + h * 16);
      const v4u x1 = *reinterpret_cast<const v4u*>(lds + L_X + xw * 512 + 256 + (lane >> 3) * 32 + h * 16);
#pragma unroll
      for (int e = 0; e < 8; ++e) {
        const v4u c = column_of(wqr + 8 * h, e);
#pragma unroll
        for (int u = 0; u < 4; ++u) {
          qa[e] = dot2(c[u], x0[u], qa[e]);
          qa[8 + e] = dot2(c[u], x1[u], qa[8 + e]);
        }
      }
    }
    float r8[8], r4[4];
#pragma unroll
    for (int e = 0; e < 8; ++e) r8[e] = swap32_add(qa[e], qa[8 + e]);
#pragma unroll
    for (int e = 0; e < 4; ++e) r4[e] = swap16_add(r8[e], r8[4 + e]);
    const bool up = lane & 8;
    const float o0 = (up ? r4[2] : r4[0]) + dpp<ROR8>(up ? r4[0] : r4[2]);
    const float o1 = (up ? r4[3] : r4[1]) + dpp<ROR8>(up ? r4[1] : r4[3]);
    *reinterpret_cast<float2*>(lds + L_QRED + xw * 512 + lane * 8) = make_float2(o0, o1);
    lds_arrive(cnt + C_QPART, lane);
  }
  if (wave == 7) {
    const u64 epoch = ((u64)(unsigned)__builtin_amdgcn_readfirstlane(ep[1]) << 32) | (unsigned)__builtin_amdgcn_readfirstlane(ep[0]);
    const u64 tg = mix64(epoch);
    const unsigned t8 = (unsigned)tg & 0xFF000000u, th = (unsigned)(tg >> 32);
    if (lane == 0) *reinterpret_cast<v4u*>(lds + L_TAG) = v4u{(unsigned)epoch, (unsigned)(epoch >> 32), t8, th};
    lds_wait(cnt + C_QPART, 4u);
    float s0 = 0.f, s1 = 0.f;
#pragma unroll
    for (int w = 0; w < 4; ++w) {
      const float2 q = *reinterpret_cast<const float2*>(lds + L_QRED + w * 512 + lane * 8);
      s0 += q.x;
      s1 += q.y;
    }
    const v4u g = {t8 | (__float_as_uint(s0) >> 8), th, t8 | (__float_as_uint(s1) >> 8), th};
    const int col = 8 * (lane & 7) + 4 * ((lane >> 4) & 1) + 2 * ((lane >> 3) & 1);
    __builtin_amdgcn_raw_buffer_store_b128(g, rws, WS_QG + (((pair * 2 + (lane >> 5)) * 2 + kh) * 1024 + qblk * 64 + col) * 8, 0, AUX_SC1);
    const unsigned t_own = uni(lds_get(cnt + C_ANCHOR));
    __builtin_amdgcn_raw_buffer_store_b64(v2u{t8 | (t_own & 0xFFFFFFu), th}, rws, lane == 0 ? WS_TIN + wg * 8 : kDrop, 0, AUX_SC1);
    lds_wait(cnt + C_RESET, 4u);
    __builtin_amdgcn_raw_buffer_store_b64(v2u{t8, th}, rws, lane == 0 ? WS_RDY + wg * 8 : kDrop, 0, AUX_SC1);
    wait_until(t_own + kTGather);
    const int qg_off = WS_QG + (pair * 2 + b) * 16384 + lane * 16;
    v4u qg[16];
    v2u tin;
    unsigned spins = 0, bad;
    do {
      MEMFENCE();
#pragma unroll
      for (int i = 0; i < 16; ++i) qg[i] = ld16<AUX_SC1>(rws, qg_off, i * 1024);
      tin = __builtin_amdgcn_raw_buffer_load_b64(rws, WS_TIN + pair * 256 + (lane & 31) * 8, 0, AUX_SC1);
      unsigned bl = tin[0] ^ t8, bh = tin[1] ^ th;
#pragma unroll
      for (int i = 0; i < 16; ++i) {
        bl |= (qg[i][0] ^ t8) | (qg[i][2] ^ t8);
        bh |= (qg[i][1] ^ th) | (qg[i][3] ^ th);
      }
      bad = (bl >> 24) | bh;
    } while (__any(bad != 0u) && ++spins < kSpinCap);
#pragma unroll
    for (int i = 0; i < 8; ++i)
      *reinterpret_cast<unsigned*>(lds + L_QIMG + i * 256 + lane * 4) =
          pack_bf16(__uint_as_float(qg[i][0] << 8) + __uint_as_float(qg[8 + i][0] << 8),
                    __uint_as_float(qg[i][2] << 8) + __uint_as_float(qg[8 + i][2] << 8));
    int lead = (int)((t_own - tin[0]) << 8) >> 8;
    lead = max(lead, __builtin_amdgcn_mov_dpp(lead, XOR1, 0xf, 0xf, true));
    lead = max(lead, __builtin_amdgcn_mov_dpp(lead, XOR2, 0xf, 0xf, true));
    lead = max(lead, __builtin_amdgcn_mov_dpp(lead, ROR4, 0xf, 0xf, true));
    lead = max(lead, __builtin_amdgcn_mov_dpp(lead, ROR8, 0xf, 0xf, true));
    lead = max(__builtin_amdgcn_readlane(lead, 0), __builtin_amdgcn_readlane(lead, 16));
    if (lane == 0) {
      lds_put(cnt + C_ANCHOR, t_own - (unsigned)min(max(lead, 0), 1000));
      lds_put(cnt + C_DUE0, t_own - (unsigned)min(max(lead, 0), 1000) + kT0 - phase);
    }
    lds_arrive(cnt + C_QIMG, lane);
  }
  PIN();
  LOAD_K(0, 0);
  LOAD_V(0, 0);
  LOAD_K(1, 0);
  LOAD_V(1, 0);
  if (!streamer || lds_get(cnt + C_QIMG) == 0u) SETTLE();
  lds_wait<1>(cnt + C_QIMG, 1u);
  const v4u tagv = *reinterpret_cast<const v4u*>(lds + L_TAG);
  const int pp = (lane >> 3) & 1, hh = lane & 7, gg = lane >> 4;
  const v4u Q0 = *reinterpret_cast<const v4u*>(lds + L_QIMG + hh * 256 + pp * 64 + gg * 16);
  const v4u Q1 = *reinterpret_cast<const v4u*>(lds + L_QIMG + hh * 256 + 128 + pp * 64 + gg * 16);

  f32x4 acc[8];
#pragma unroll
  for (int t = 0; t < 8; ++t) acc[t] = f32x4{0.f, 0.f, 0.f, 0.f};
  float m = -INFINITY, l = 0.f;
  const unsigned pmask = pp ? 0xFFFF0000u : 0x0000FFFFu;
#define TILE(buf, AFTER_QK, AFTER_PV)                                                                                \
  do {                                                                                                               \
    float s[4];                                                                                                      \
    _Pragma("unroll") for (int T = 0; T < 4; ++T) {                                                                  \
      f32x4 d = mfma16(Kt[buf][2 * T], Q0, f32x4{0.f, 0.f, 0.f, 0.f});                                               \
      d = mfma16(Kt[buf][2 * T + 1], Q1, d);                                                                         \
      s[T] = (pp ? d[3] : d[0]) + dpp<ROR8>(pp ? d[1] : d[2]);                                                       \
    }                                                                                                                \
    PIN();                                                                                                           \
    AFTER_QK;                                                                                                        \
    float mt = fmaxf(fmaxf(s[0], s[1]), fmaxf(s[2], s[3]));                                                          \
    mt = fmaxf(mt, dpp<ROR8>(mt));                                                                                   \
    mt = swap32_max(swap16_max(mt)) * kC2;                                                                           \
    const float mn = fmaxf(m, mt);                                                                                   \
    if (__any(mn != m)) {                                                                                            \
      const float alpha = __builtin_amdgcn_exp2f(m - mn);                                                            \
      l *= alpha;                                                                                                    \
      _Pragma("unroll") for (int i = 0; i < 8; ++i) acc[i] *= alpha;                                                 \
      m = mn;                                                                                                        \
    }                                                                                                                \
    v4u P;                                                                                                           \
    _Pragma("unroll") for (int T = 0; T < 4; ++T) {                                                                  \
      const float e = __builtin_amdgcn_exp2f(fmaf(s[T], kC2, -m));                                                   \
      l += e;                                                                                                        \
      P[T] = pack_bf16(e, e) & pmask;                                                                                \
    }                                                                                                                \
    _Pragma("unroll") for (int i = 0; i < 8; ++i) acc[i] = mfma16(column_of(Vt[buf], i), P, acc[i]);                 \
    PIN();                                                                                                           \
    AFTER_PV;                                                                                                        \
  } while (0)
  const int nunit = streamer ? kUnitsS : 1;
#pragma unroll 1
  for (int u = 1; u < nunit; ++u) {
    TILE(0, LOAD_K(0, u), LOAD_V(0, u));
    TILE(1, LOAD_K(1, u), LOAD_V(1, u));
  }
  v4u rdy = ld16<AUX_SC1>(rws, wave < 2 ? lane * 16 : kDrop, WS_RDY + br * 1024);
  PIN();
  if (streamer) lds_arrive(cnt + C_LAST, lane);
  PIN();
  TILE(0, PIN(), PIN());
  TILE(1, PIN(), PIN());
#undef TILE
#undef LOAD_K
#undef LOAD_V
#undef GATE

  l += dpp<ROR8>(l);
  l = swap16_add(l, l);
  l = swap32_add(l, l);
  {
    float* dst = reinterpret_cast<float*>(lds + L_MRG) + (wave * 8 + hh) * 132 + 32 * gg + 16 * pp;
#pragma unroll
    for (int rr = 0; rr < 2; ++rr)
#pragma unroll
      for (int q = 0; q < 2; ++q) {
        f32x4 o;
#pragma unroll
        for (int e = 0; e < 4; ++e) {
          const f32x4 a = acc[4 * q + e];
          o[e] = (pp ? a[2 + rr] : a[rr]) + dpp<ROR8>(pp ? a[rr] : a[2 + rr]);
        }
        *reinterpret_cast<f32x4*>(dst + 8 * rr + 4 * q) = o;
      }
    if (lane < 8) {
      float* ml = reinterpret_cast<float*>(lds + L_MRGML) + lane * 16;
      ml[wave] = m;
      ml[8 + wave] = l;
    }
  }
  SETTLE();
  lds_arrive(cnt + C_PART, lane);
  if (streamer) {
    lds_wait(cnt + C_PART, 8u);
    {
      const int hd = 2 * wave + (lane >> 5);
      const float* ml = reinterpret_cast<const float*>(lds + L_MRGML) + hd * 16;
      const f32x4 m0 = *reinterpret_cast<const f32x4*>(ml), m1 = *reinterpret_cast<const f32x4*>(ml + 4);
      const f32x4 l0 = *reinterpret_cast<const f32x4*>(ml + 8), l1 = *reinterpret_cast<const f32x4*>(ml + 12);
      f32x4 o[8];
#pragma unroll
      for (int w = 0; w < 8; ++w) o[w] = *reinterpret_cast<const f32x4*>(lds + L_MRG + ((w * 8 + hd) * 132 + 4 * (lane & 31)) * 4);
      const float M = fmaxf(fmaxf(fmaxf(m0[0], m0[1]), fmaxf(m0[2], m0[3])), fmaxf(fmaxf(m1[0], m1[1]), fmaxf(m1[2], m1[3])));
      float L = 0.f;
      f32x4 os = {0.f, 0.f, 0.f, 0.f};
#pragma unroll
      for (int w = 0; w < 8; ++w) {
        const float e = __builtin_amdgcn_exp2f((w < 4 ? m0[w & 3] : m1[w & 3]) - M);
        L = fmaf(e, w < 4 ? l0[w & 3] : l1[w & 3], L);
        os += e * o[w];
      }
      const float inv = __builtin_amdgcn_rcpf(L);
      const unsigned u0 = quiet(os[0] * inv), u1 = quiet(os[1] * inv), u2 = quiet(os[2] * inv), u3 = quiet(os[3] * inv);
      const v4u ov = {quiet(M + __builtin_amdgcn_logf(L)), __builtin_amdgcn_perm(u0, u1, 0x07060501u),
                      __builtin_amdgcn_perm(u2, u1, 0x07060302u), __builtin_amdgcn_perm(u3, u2, 0x07060501u)};
      __builtin_amdgcn_raw_buffer_store_b128(ov, rws, WS_PART + wg * kPartSlot + wave * 1024 + lane * 16, 0, AUX_SC1);
    }
    PIN();
    if (wave < 2) {
#define RDY_BAD() __any(((rdy[0] ^ tagv[2]) | (rdy[1] ^ tagv[3]) | (rdy[2] ^ tagv[2]) | (rdy[3] ^ tagv[3])) != 0u)
      if (RDY_BAD()) {
        unsigned spins = 0;
        do {
          nap(kNapPoll);
          rdy = ld16<AUX_SC1>(rws, lane * 16, WS_RDY + br * 1024);
        } while (RDY_BAD() && ++spins < kSpinCap);
      }
#undef RDY_BAD
      const int po = WS_PART + (pair * 32 + wave * 16 + (lane >> 3)) * kPartSlot + idx * 128 + 16 * (lane & 7);
      nap(kNap1);
      v4u pa = ld16<AUX_SC1>(rws, po, 0), pb = ld16<AUX_SC1>(rws, po, 8 * kPartSlot);
      for (unsigned spins = 0;; ++spins) {
        const bool ok = max(umax4(pa), umax4(pb)) != kSent;
        if (__all(ok) || spins > kSpinCap) break;
        nap(kNapPoll);
        if (!ok) {
          pa = ld16<AUX_SC1>(rws, po, 0);
          pb = ld16<AUX_SC1>(rws, po, 8 * kPartSlot);
        }
      }
      const float la = __uint_as_float(pa[0]), lb = __uint_as_float(pb[0]);
      float M = fmaxf(la, lb);
      M = fmaxf(M, dpp<ROR8>(M));
      M = swap32_max(swap16_max(M));
      const float ea = __builtin_amdgcn_exp2f(la - M), eb = __builtin_amdgcn_exp2f(lb - M);
      float v[5] = {ea + eb,
                    ea * __uint_as_float(pa[1] & 0xFFFFFF00u) + eb * __uint_as_float(pb[1] & 0xFFFFFF00u),
                    ea * __uint_as_float(__builtin_amdgcn_perm(pa[2], pa[1], 0x0504000Cu)) +
                        eb * __uint_as_float(__builtin_amdgcn_perm(pb[2], pb[1], 0x0504000Cu)),
                    ea * __uint_as_float(__builtin_amdgcn_perm(pa[2], pa[3], 0x0706000Cu)) +
                        eb * __uint_as_float(__builtin_amdgcn_perm(pb[2], pb[3], 0x0706000Cu)),
                    ea * __uint_as_float(pa[3] & 0xFFFFFF00u) + eb * __uint_as_float(pb[3] & 0xFFFFFF00u)};
#pragma unroll
      for (int i = 0; i < 5; ++i) v[i] += dpp<ROR8>(v[i]);
#pragma unroll
      for (int i = 0; i < 5; ++i) v[i] = swap16_add(v[i], v[i]);
#pragma unroll
      for (int i = 0; i < 5; ++i) v[i] = swap32_add(v[i], v[i]);
      const float inv = __builtin_amdgcn_rcpf(v[0]);
      const v2u ob = {pack_bf16(v[1] * inv, v[2] * inv), pack_bf16(v[3] * inv, v[4] * inv)};
      if (lane < 8) *reinterpret_cast<v2u*>(lds + L_OIMG + wave * 64 + lane * 8) = ob;
    }
    wg_barrier();
    if (wave != 0) return;
  } else {
    if (wave == 6) {
      const unsigned anchor = uni(lds_get(cnt + C_ANCHOR)), t8 = uni(tagv[2]), th = uni(tagv[3]);
      const unsigned t_give = anchor + kMarkAt + kMarkWait;
      while (lds_get(cnt + C_MARK2) == 0u && (int)(now_ticks() - t_give) < 0) __builtin_amdgcn_s_sleep(4);
      const unsigned m1 = lds_get(cnt + C_MARK1), m2r = lds_get(cnt + C_MARK2), m2 = m2r ? m2r : t_give;
      const unsigned span = m1 ? min(m2 - m1, 4095u) : 4095u;
      const bool tell = m2 - anchor > kMarkAt + kQuietLag || span * 100u > kMarkSpan * 103u;
      __builtin_amdgcn_raw_buffer_store_b64(v2u{t8 | (span << 12) | min(m2 - anchor, 4095u), th}, rws,
                                            lane == 0 && tell ? WS_LAG + wg * 8 : kDrop, 0, AUX_SC1);
      wait_until(t_give + 120u);
      const v2u lg = __builtin_amdgcn_raw_buffer_load_b64(rws, WS_LAG + pair * 256 + (lane & 31) * 8, 0, AUX_SC1);
      const bool none = (((lg[0] ^ t8) >> 24) | (lg[1] ^ th)) != 0u;
      float sp = (float)(none ? kMarkSpan : (lg[0] >> 12) & 4095u), at = (float)(none ? kMarkAt + kUsualLag : lg[0] & 4095u);
      sp += dpp<XOR1>(sp);
      at += dpp<XOR1>(at);
      sp += dpp<XOR2>(sp);
      at += dpp<XOR2>(at);
      sp += dpp<ROR4>(sp);
      at += dpp<ROR4>(at);
      sp += dpp<ROR8>(sp);
      at += dpp<ROR8>(at);
      sp = swap16_add(sp, sp) * (1.f / (32.f * (float)(kMark2 - kMark1)));
      at = swap16_add(at, at) * (1.f / 32.f);
      const float sl = fminf(fmaxf(1.005f * sp, ((float)kTau16 + 0.4f * (at - (float)(kMarkAt + 130))) / 16.f), 32.5f);
      if (sl > 1.02f * (float)kTau16 / 16.f && lane == 0)
        line_put(cnt + C_DUE0, anchor + (unsigned)(int)(at - (float)kMark2 * sl) - 44u - phase, (unsigned)(sl * 64.f));
    }
    lds_wait<2>(cnt + C_LAST, 4u);
    const unsigned t_wo = now_ticks();
    PIN();
    v4u wor[16], wc[16];
#pragma unroll
    for (int i = 0; i < 16; ++i) {
      if ((i & 7) == 0) {
        unsigned tk = 0;
        if (lane == 0) tk = __hip_atomic_fetch_add(cnt + C_TICKET, 1u, __ATOMIC_RELAXED, __HIP_MEMORY_SCOPE_WORKGROUP);
        tk = uni(tk);
        const u64 ln = line_get(cnt + C_DUE0);
        const unsigned sl = uni((unsigned)(ln >> 32));
        wait_until(uni((unsigned)ln) + ((tk * sl) >> 6));
        wait_until(t_wo + (((max(tk, 128u) - 128u) * sl) >> 6));
      }
      wor[i] = ld16<AUX_NT>(ro, gg * 8 * 2048 + (lane & 15) * 16, (i & 7) * 2048 + (i >> 3) * 256 + (wave & 3) * 512);
    }
    PIN();
#pragma unroll
    for (int e = 0; e < 16; ++e) {
      wc[e] = column_of(wor + (e & 8), e & 7);
      asm volatile("" : "+v"(wc[e]));
    }
    int ba[8];
#pragma unroll
    for (int e = 0; e < 8; ++e) {
      ba[e] = ((lane & 15) >> 1) == e ? L_OIMG + (lane & 1) * 64 + gg * 16 : L_ZERO;
      asm volatile("" : "+v"(ba[e]));
    }
    wg_barrier();
    v4u bf[8];
#pragma unroll
    for (int e = 0; e < 8; ++e) bf[e] = *reinterpret_cast<const v4u*>(lds + ba[e]);
    f32x4 d0 = {0.f, 0.f, 0.f, 0.f}, d1 = {0.f, 0.f, 0.f, 0.f};
#pragma unroll
    for (int e = 0; e < 8; ++e) {
      d0 = mfma16(wc[e], bf[e], d0);
      d1 = mfma16(wc[8 + e], bf[e], d1);
    }
    const v4u o0 = {not_sentinel(d0[0]), not_sentinel(d0[1]), not_sentinel(d0[2]), not_sentinel(d0[3])};
    const v4u o1 = {not_sentinel(d1[0]), not_sentinel(d1[1]), not_sentinel(d1[2]), not_sentinel(d1[3])};
    const int so = WS_OP + wg * 8192 + (wave & 3) * 2048 + lane * 16;
    __builtin_amdgcn_raw_buffer_store_b128(o0, rws, so, 0, AUX_SC1);
    __builtin_amdgcn_raw_buffer_store_b128(o1, rws, so + 1024, 0, AUX_SC1);
    return;
  }
#undef SETTLE
#undef REPORT

  const int r = wg & 127;
  const int fo = WS_OP + (br * 128 + (lane >> 2)) * 8192 + r * 64 + (lane & 3) * 16;
  const int pos = 4 * r + (lane & 3), ln = pos & 63;
  unsigned short* dst = (unsigned short*)a10 + (ln & 1) * 2048 + br * 1024 + 128 * (pos >> 6) + 32 * (ln >> 4) + 8 * (lane >> 4) + ((ln & 15) >> 1);
  asm volatile("" : "+v"(dst));
  v4u f[8];
  nap(kNap2);
#pragma unroll
  for (int q = 0; q < 8; ++q) f[q] = ld16<AUX_SC1>(rws, fo, q * 16 * 8192);
  for (unsigned spins = 0;; ++spins) {
    unsigned mx = 0u;
#pragma unroll
    for (int q = 0; q < 8; ++q) mx = max(mx, umax4(f[q]));
    const bool ok = mx != kSent;
    if (__all(ok) || spins > kSpinCap) break;
    nap(kNapPoll);
    if (!ok) {
#pragma unroll
      for (int q = 0; q < 8; ++q) f[q] = ld16<AUX_SC1>(rws, fo, q * 16 * 8192);
    }
  }
  float y[4];
#pragma unroll
  for (int e = 0; e < 4; ++e)
    y[e] = ((__uint_as_float(f[0][e]) + __uint_as_float(f[1][e])) + (__uint_as_float(f[2][e]) + __uint_as_float(f[3][e]))) +
           ((__uint_as_float(f[4][e]) + __uint_as_float(f[5][e])) + (__uint_as_float(f[6][e]) + __uint_as_float(f[7][e])));
  const float z0 = swap32_add(y[0], y[2]), z1 = swap32_add(y[1], y[3]);
  float z = swap16_add(z0, z1);
  z += dpp<ROR4>(z);
  z += dpp<ROR8>(z);
  if ((lane & 12) == 0) *(__attribute__((address_space(1))) unsigned short*)dst = (unsigned short)(pack_bf16(z, z) & 0xFFFFu);
  if (r == 0 && lane == 0) {
    const u64 nx = (((u64)tagv[1] << 32) | tagv[0]) + 1;
    __builtin_amdgcn_raw_buffer_store_b64(v2u{(unsigned)nx, (unsigned)(nx >> 32)}, rws, WS_EPOCH + br * 256, 0, AUX_SC1);
  }
}

}

extern "C" void kernel_launch(void* const* d_in, const int* in_sizes, int n_in, void* d_out, int out_size, void* d_ws,
                              size_t ws_size, hipStream_t stream) {
  (void)in_sizes; (void)n_in; (void)out_size; (void)ws_size;
  Params p;
  for (int i = 0; i < 2; ++i) {
    p.x[i] = (const unsigned short*)d_in[5 * i + 0];
    p.k[i] = (const unsigned short*)d_in[5 * i + 1];
    p.v[i] = (const unsigned short*)d_in[5 * i + 2];
    p.wq[i] = (const unsigned short*)d_in[5 * i + 3];
    p.wo[i] = (const unsigned short*)d_in[5 * i + 4];
  }
  p.out = (unsigned short*)d_out;
  p.ws = (unsigned char*)d_ws;
  hipLaunchKernelGGL(fused_decode, dim3(kNWG), dim3(kNThr), 0, stream, p);
}
```

```cpp
#include <hip/hip_runtime.h>
#include <hip/hip_bf16.h>
#include <stdint.h>

typedef unsigned v4u __attribute__((ext_vector_type(4)));
typedef unsigned v2u __attribute__((ext_vector_type(2)));
typedef float f32x4 __attribute__((ext_vector_type(4)));
typedef __bf16 bf16x8 __attribute__((ext_vector_type(8)));
typedef __bf16 bf16x2 __attribute__((ext_vector_type(2)));
typedef unsigned long long u64;
typedef __amdgpu_buffer_rsrc_t rsrc_t;

namespace {

constexpr int kD = 1024, kHkv = 4, kSk = 32768, kDh = 128;
constexpr int kNWG = 256, kNThr = 512;
constexpr int kUnitsS = 7;
constexpr int AUX_NT = 2, AUX_SC1 = 16;
constexpr int RSRC_W3 = 0x00020000;
constexpr unsigned kSent = 0xFFFFFFFFu;
constexpr int kDrop = 0x7FFFFF00;
constexpr unsigned kSpinCap = 1u << 18;
constexpr float kC2 = 0.088388347648318440f * 1.4426950408889634f;

constexpr unsigned kT0 = 400;
constexpr unsigned kTau16 = 450;
constexpr int kWin16 = 52, kWinGain = 35;
constexpr int kLate = 100;
constexpr unsigned kMark1 = 44, kMark2 = 68;
constexpr unsigned kMarkWait = 250;
constexpr unsigned kMarkAt = kT0 + ((kMark2 * kTau16) >> 4), kMarkSpan = ((kMark2 - kMark1) * kTau16) >> 4;
constexpr unsigned kQuietLag = 150, kUsualLag = 90;
constexpr unsigned kTGather = 540;
constexpr unsigned kNap1 = 70, kNap2 = 95, kNapPoll = 20;

constexpr int WS_EPOCH = 0;
constexpr int WS_RDY = 2048;
constexpr int WS_TIN = 4096;
constexpr int WS_LAG = 6144;
constexpr int WS_QG = 8192;
constexpr int kPartSlot = 4096;
constexpr int WS_PART = WS_QG + 8 * 2 * 2 * 1024 * 8;
constexpr int WS_OP = WS_PART + 256 * kPartSlot;
constexpr int WS_END = WS_OP + 256 * 8192;

constexpr int L_X = 0;
constexpr int L_TAG = L_X + 2048;
constexpr int L_CNT = L_TAG + 16;
constexpr int L_QRED = L_CNT + 144;
constexpr int L_QIMG = L_QRED + 4 * 512;
constexpr int L_OIMG = L_QIMG + 2048;
constexpr int L_ZERO = L_OIMG + 128;
constexpr int L_MRGML = L_ZERO + 16;
constexpr int L_MRG = L_MRGML + 8 * 16 * 4;
constexpr int L_END = L_MRG + 8 * 8 * 132 * 4;
static_assert(L_QRED % 16 == 0 && L_QIMG % 16 == 0 && L_OIMG % 16 == 0 && L_MRGML % 16 == 0 && L_MRG % 16 == 0, "LDS alignment");

enum {
  C_QPART, C_QIMG, C_RESET, C_LAST, C_PART,
  C_TICKET, C_LANDED, C_ANCHOR,
  C_PROG = 8,
  C_DUE0 = 12, C_SLOPE, C_MARK1, C_MARK2,
  C_RING = 16,
  C_RINGPOS = 32,
  C_WORDS
};

struct Params {
  const unsigned short* x[2];
  const unsigned short* k[2];
  const unsigned short* v[2];
  const unsigned short* wq[2];
  const unsigned short* wo[2];
  unsigned short* out;
  unsigned char* ws;
};

__device__ __forceinline__ rsrc_t make_rsrc(const void* p, int bytes) {
  return __builtin_amdgcn_make_buffer_rsrc((void*)p, 0, bytes, RSRC_W3);
}
template <int AUX> __device__ __forceinline__ v4u ld16(rsrc_t r, int voff, int soff) {
  return __builtin_amdgcn_raw_buffer_load_b128(r, voff, soff, AUX);
}
__device__ __forceinline__ f32x4 mfma16(v4u a, v4u b, f32x4 c) {
  return __builtin_amdgcn_mfma_f32_16x16x32_bf16(__builtin_bit_cast(bf16x8, a), __builtin_bit_cast(bf16x8, b), c, 0, 0, 0);
}
__device__ __forceinline__ float dot2(unsigned a, unsigned b, float c) {
  return __builtin_amdgcn_fdot2_f32_bf16(__builtin_bit_cast(bf16x2, a), __builtin_bit_cast(bf16x2, b), c, false);
}
__device__ __forceinline__ unsigned pack_bf16(float lo, float hi) {
  bf16x2 r = {(__bf16)lo, (__bf16)hi};
  return __builtin_bit_cast(unsigned, r);
}
template <int CTRL> __device__ __forceinline__ float dpp(float x) {
  return __builtin_bit_cast(float, __builtin_amdgcn_mov_dpp(__builtin_bit_cast(int, x), CTRL, 0xf, 0xf, true));
}
constexpr int XOR1 = 0xB1, XOR2 = 0x4E, ROR4 = 0x124, ROR8 = 0x128;
__device__ __forceinline__ float swap16_add(float a, float b) {
  auto s = __builtin_amdgcn_permlane16_swap(__float_as_uint(a), __float_as_uint(b), false, false);
  return __uint_as_float(s[0]) + __uint_as_float(s[1]);
}
__device__ __forceinline__ float swap32_add(float a, float b) {
  auto s = __builtin_amdgcn_permlane32_swap(__float_as_uint(a), __float_as_uint(b), false, false);
  return __uint_as_float(s[0]) + __uint_as_float(s[1]);
}
__device__ __forceinline__ float swap16_max(float a) {
  auto s = __builtin_amdgcn_permlane16_swap(__float_as_uint(a), __float_as_uint(a), false, false);
  return fmaxf(__uint_as_float(s[0]), __uint_as_float(s[1]));
}
__device__ __forceinline__ float swap32_max(float a) {
  auto s = __builtin_amdgcn_permlane32_swap(__float_as_uint(a), __float_as_uint(a), false, false);
  return fmaxf(__uint_as_float(s[0]), __uint_as_float(s[1]));
}
__device__ __forceinline__ v4u column_of(const v4u* r, int t) {
  v4u o;
#pragma unroll
  for (int u = 0; u < 4; ++u)
    o[u] = __builtin_amdgcn_perm(r[2 * u + 1][t >> 1], r[2 * u][t >> 1], (t & 1) ? 0x07060302u : 0x05040100u);
  return o;
}
__device__ __forceinline__ unsigned not_sentinel(float f) { return min(__float_as_uint(f), kSent - 1u); }
__device__ __forceinline__ unsigned quiet(float f) { return f != f ? 0x7FC00000u : __float_as_uint(f); }
__device__ __forceinline__ unsigned umax4(v4u a) { return max(max(a[0], a[1]), max(a[2], a[3])); }
__device__ __forceinline__ u64 mix64(u64 z) {
  z += 0x9E3779B97F4A7C15ull;
  z = (z ^ (z >> 30)) * 0xBF58476D1CE4E5B9ull;
  z = (z ^ (z >> 27)) * 0x94D049BB133111EBull;
  return z ^ (z >> 31);
}
#define PIN() __builtin_amdgcn_sched_barrier(0)
#define MEMFENCE() asm volatile("" ::: "memory")
__device__ __forceinline__ void wg_barrier() {
  asm volatile("s_waitcnt lgkmcnt(0)" ::: "memory");
  __builtin_amdgcn_s_barrier();
  MEMFENCE();
}
__device__ __forceinline__ void lds_arrive(unsigned* c, int lane) {
  asm volatile("s_waitcnt lgkmcnt(0)" ::: "memory");
  if (lane == 0) __hip_atomic_fetch_add(c, 1u, __ATOMIC_RELAXED, __HIP_MEMORY_SCOPE_WORKGROUP);
}
template <int SLEEP = 0> __device__ __forceinline__ void lds_wait(unsigned* c, unsigned n) {
  while (__hip_atomic_load(c, __ATOMIC_RELAXED, __HIP_MEMORY_SCOPE_WORKGROUP) < n) __builtin_amdgcn_s_sleep(SLEEP);
  MEMFENCE();
}
__device__ __forceinline__ unsigned now_ticks() { return (unsigned)__builtin_amdgcn_s_memrealtime(); }
__device__ __forceinline__ void wait_until(unsigned t) {
  while ((int)(now_ticks() - t) < 0) __builtin_amdgcn_s_sleep(1);
  MEMFENCE();
}
__device__ __forceinline__ void nap(unsigned ticks) { wait_until(now_ticks() + ticks); }
__device__ __forceinline__ unsigned lds_get(unsigned* c) {
  return __hip_atomic_load(c, __ATOMIC_RELAXED, __HIP_MEMORY_SCOPE_WORKGROUP);
}
__device__ __forceinline__ void lds_put(unsigned* c, unsigned v) {
  __hip_atomic_store(c, v, __ATOMIC_RELAXED, __HIP_MEMORY_SCOPE_WORKGROUP);
}
__device__ __forceinline__ unsigned uni(unsigned v) { return __builtin_amdgcn_readfirstlane(v); }
__device__ __forceinline__ u64 line_get(unsigned* c) {
  return __hip_atomic_load(reinterpret_cast<u64*>(c), __ATOMIC_RELAXED, __HIP_MEMORY_SCOPE_WORKGROUP);
}
__device__ __forceinline__ void line_put(unsigned* c, unsigned due0, unsigned slope) {
  __hip_atomic_store(reinterpret_cast<u64*>(c), ((u64)slope << 32) | due0, __ATOMIC_RELAXED, __HIP_MEMORY_SCOPE_WORKGROUP);
}

__global__ void __launch_bounds__(kNThr, 2) fused_decode(Params p) {
  __shared__ __attribute__((aligned(16))) unsigned char lds[L_END];
  const int tid = threadIdx.x, lane = tid & 63;
  const int wave = __builtin_amdgcn_readfirstlane(tid >> 6);
  const bool streamer = wave < 4;
  const int wg = blockIdx.x, pair = wg >> 5, idx = wg & 31;
  const int br = pair >> 2, kvh = pair & 3, b = idx >> 4, split = idx & 15;
  const int qblk = idx & 15, kh = idx >> 4;
  u64 a0 = (u64)p.x[0], a1 = (u64)p.x[1], a2 = (u64)p.k[0], a3 = (u64)p.k[1], a4 = (u64)p.v[0], a5 = (u64)p.v[1];
  u64 a6 = (u64)p.wq[0], a7 = (u64)p.wq[1], a8 = (u64)p.wo[0], a9 = (u64)p.wo[1], a10 = (u64)p.out, a11 = (u64)p.ws;
  const unsigned t_in = now_ticks();
  asm volatile("" : "+s"(a0), "+s"(a1), "+s"(a2), "+s"(a3), "+s"(a4), "+s"(a5), "+s"(a6), "+s"(a7), "+s"(a8),
               "+s"(a9), "+s"(a10), "+s"(a11));
  const unsigned short* const px = (const unsigned short*)(br ? a1 : a0);
  const unsigned short* const pk = (const unsigned short*)(br ? a3 : a2);
  const unsigned short* const pv = (const unsigned short*)(br ? a5 : a4);
  const unsigned short* const pwq = (const unsigned short*)(br ? a7 : a6);
  const unsigned short* const pwo = (const unsigned short*)(br ? a9 : a8);
  PIN();
  const rsrc_t rws = make_rsrc((const void*)a11, WS_END);
  const rsrc_t ro = make_rsrc(pwo + (size_t)(kvh * 1024 + idx * 32) * kD, 32 * kD * 2);
  const size_t kv_off = ((size_t)(b * kHkv + kvh) * kSk + (size_t)split * 2048) * kDh;
  const rsrc_t rk = make_rsrc(pk + kv_off, 2048 * kDh * 2);
  const rsrc_t rv = make_rsrc(pv + kv_off, 2048 * kDh * 2);
  const int toff0 = ((wave + wg + (wg >> 3) - (streamer ? 0 : 4)) & 7) * 8192, toff1 = toff0 ^ 32768;
  const int blk0 = (wg >> 3) + (wg >> 6) * 3 + (streamer ? 0 : 7);
  const unsigned phase = (wg * 37) & 31;

  unsigned* const cnt = reinterpret_cast<unsigned*>(lds + L_CNT);
  v4u wqr[16], xv;
  v2u ep;
  if (!streamer) {
    const int xw = wave - 4;
    const rsrc_t rq = make_rsrc(pwq + (size_t)(kh * 512 + xw * 128) * 4096 + kvh * 1024 + qblk * 64, 127 * 8192 + 128);
    const rsrc_t rx = make_rsrc(px + kh * 512 + xw * 128, 2048 + 256);
    const int qo = (lane >> 3) * 16 * 8192 + (lane & 7) * 16;
    ep = __builtin_amdgcn_raw_buffer_load_b64(rws, wave == 7 ? WS_EPOCH + br * 256 : kDrop, 0, AUX_SC1);
    xv = ld16<0>(rx, lane < 32 ? (lane >> 4) * 2048 + (lane & 15) * 16 : kDrop, 0);
#pragma unroll
    for (int i = 0; i < 16; ++i) wqr[i] = ld16<AUX_NT>(rq, qo, i * 8192);
    if (tid < 256 + C_WORDS) cnt[lane] = lane == C_ANCHOR ? t_in : lane == C_DUE0 ? t_in + kT0 : lane == C_SLOPE ? 4u * kTau16 : 0u;
    if (tid == 320) *reinterpret_cast<v4u*>(lds + L_ZERO) = v4u{0u, 0u, 0u, 0u};
  }
  PIN();
  wg_barrier();
  PIN();
  const int kl = (lane & 15) >> 1;
  int koff = (4 * (kl & 1) + (kl >> 1)) * 256 + (4 * (lane & 1) + (lane >> 4)) * 16;
  int voff = lane * 16;
  v4u Kt[2][8], Vt[2][8];
  bool owes = false;
  unsigned ngrp = 0;
#define REPORT()                                                                                                     \
  do {                                                                                                               \
    if (lane == 0) {                                                                                                 \
      const unsigned i_ = __hip_atomic_fetch_add(cnt + C_RINGPOS, 1u, __ATOMIC_RELAXED, __HIP_MEMORY_SCOPE_WORKGROUP); \
      lds_put(cnt + C_RING + (i_ & 15u), now_ticks());                                                               \
    }                                                                                                                \
    lds_arrive(cnt + C_LANDED, lane);                                                                                \
    owes = false;                                                                                                    \
  } while (0)
#define GATE()                                                                                                       \
  do {                                                                                                               \
    if (streamer) {                                                                                                  \
      for (int w_ = 0; w_ < 200; ++w_) {                                                                             \
        const unsigned a_ = min(min(lds_get(cnt + C_PROG), lds_get(cnt + C_PROG + 1)),                               \
                                min(lds_get(cnt + C_PROG + 2), lds_get(cnt + C_PROG + 3)));                          \
        if (ngrp <= uni(a_)) break;                                                                                  \
        __builtin_amdgcn_s_sleep(1);                                                                                 \
      }                                                                                                              \
      ++ngrp;                                                                                                        \
      if (lane == 0) lds_put(cnt + C_PROG + wave, ngrp);                                                             \
    }                                                                                                                \
    unsigned tk_ = 0;                                                                                                \
    if (lane == 0) tk_ = __hip_atomic_fetch_add(cnt + C_TICKET, 1u, __ATOMIC_RELAXED, __HIP_MEMORY_SCOPE_WORKGROUP); \
    tk_ = uni(tk_);                                                                                                  \
    const u64 ln_ = line_get(cnt + C_DUE0);                                                                          \
    const unsigned sl_ = uni((unsigned)(ln_ >> 32)), due_ = uni((unsigned)ln_) + ((tk_ * sl_) >> 6);                 \
    if (owes) {                                                                                                      \
      if ((int)(now_ticks() - due_) < kLate) asm volatile("s_waitcnt vmcnt(0)" ::: "memory");                        \
      else asm volatile("s_waitcnt vmcnt(4)" ::: "memory");                                                          \
      REPORT();                                                                                                      \
    }                                                                                                                \
    owes = true;                                                                                                     \
    for (;;) {                                                                                                       \
      const unsigned now_ = now_ticks();                                                                             \
      const int late_ = (int)(now_ - due_);                                                                          \
      if (late_ >= 0) {                                                                                              \
        const int c16_ = kWin16 + ((late_ * kWinGain) >> 8);                                                         \
        const int n_ = (c16_ + 15) >> 4, need_ = (int)tk_ - n_;                                                      \
        if (need_ < 0) break;                                                                                        \
        const int landed_ = (int)uni(lds_get(cnt + C_LANDED));                                                       \
        if (landed_ > need_ + 1) break;                                                                              \
        if (landed_ > need_ &&                                                                                       \
            (int)(now_ - uni(lds_get(cnt + C_RING + (need_ & 15)))) >= (int)(((n_ * 16 - c16_) * sl_) >> 10))        \
          break;                                                                                                     \
      }                                                                                                              \
      __builtin_amdgcn_s_sleep(1);                                                                                   \
    }                                                                                                                \
    if ((tk_ == kMark1 || tk_ == kMark2) && lane == 0) lds_put(cnt + (tk_ == kMark1 ? C_MARK1 : C_MARK2), now_ticks() | 1u); \
    MEMFENCE();                                                                                                      \
  } while (0)
#define SETTLE()                                                                                                     \
  do {                                                                                                               \
    if (owes) {                                                                                                      \
      asm volatile("s_waitcnt vmcnt(0)" ::: "memory");                                                               \
      REPORT();                                                                                                      \
    }                                                                                                                \
  } while (0)
#define LOAD_K(buf, u)                                                                                               \
  do {                                                                                                               \
    GATE();                                                                                                          \
    asm volatile("" : "+v"(koff));                                                                                   \
    const int so_ = ((blk0 + (u)) & 7) * 65536 + ((buf) ? toff1 : toff0);                                            \
    _Pragma("unroll") for (int i = 0; i < 8; ++i)                                                                    \
        Kt[buf][i] = ld16<AUX_NT>(rk, koff + (i & 2) * 1024 + (i & 1) * 128, so_ + (i >> 2) * 4096);                 \
    PIN();                                                                                                           \
  } while (0)
#define LOAD_V(buf, u)                                                                                               \
  do {                                                                                                               \
    GATE();                                                                                                          \
    asm volatile("" : "+v"(voff));                                                                                   \
    const int so_ = ((blk0 + (u)) & 7) * 65536 + ((buf) ? toff1 : toff0);                                            \
    _Pragma("unroll") for (int i = 0; i < 8; ++i)                                                                    \
        Vt[buf][i] = ld16<AUX_NT>(rv, voff + (i & 3) * 1024, so_ + (i >> 2) * 4096);                                 \
    PIN();                                                                                                           \
  } while (0)

  if (streamer) {
    const int to = lane == 0 ? 0 : kDrop;
    const unsigned t0 = __builtin_amdgcn_raw_buffer_load_b32(rk, to, wave * 131072, AUX_NT);
    const unsigned t1 = __builtin_amdgcn_raw_buffer_load_b32(rv, to, wave * 131072, AUX_NT);
    const unsigned t2 = __builtin_amdgcn_raw_buffer_load_b32(ro, to, wave * 16384, AUX_NT);
    asm volatile("" ::"v"(t0), "v"(t1), "v"(t2));
    const v4u s4 = {kSent, kSent, kSent, kSent};
    __builtin_amdgcn_raw_buffer_store_b128(s4, rws, WS_OP + wg * 8192 + tid * 16, 0, AUX_SC1);
    __builtin_amdgcn_raw_buffer_store_b128(s4, rws, WS_OP + wg * 8192 + 4096 + tid * 16, 0, AUX_SC1);
    __builtin_amdgcn_raw_buffer_store_b128(s4, rws, WS_PART + wg * kPartSlot + tid * 16, 0, AUX_SC1);
    asm volatile("s_waitcnt vmcnt(0)" ::: "memory");
    lds_arrive(cnt + C_RESET, lane);
  } else {
    const int xw = wave - 4;
    if (lane < 32) *reinterpret_cast<v4u*>(lds + L_X + xw * 512 + lane * 16) = xv;
    asm volatile("s_waitcnt lgkmcnt(0)" ::: "memory");
    float qa[16];
#pragma unroll
    for (int i = 0; i < 16; ++i) qa[i] = 0.f;
#pragma unroll
    for (int h = 0; h < 2; ++h) {
      const v4u x0 = *reinterpret_cast<const v4u*>(lds + L_X + xw * 512 + (lane >> 3) * 32 + h * 16);
      const v4u x1 = *reinterpret_cast<const v4u*>(lds + L_X + xw * 512 + 256 + (lane >> 3) * 32 + h * 16);
#pragma unroll
      for (int e = 0; e < 8; ++e) {
        const v4u c = column_of(wqr + 8 * h, e);
#pragma unroll
        for (int u = 0; u < 4; ++u) {
          qa[e] = dot2(c[u], x0[u], qa[e]);
          qa[8 + e] = dot2(c[u], x1[u], qa[8 + e]);
        }
      }
    }
    float r8[8], r4[4];
#pragma unroll
    for (int e = 0; e < 8; ++e) r8[e] = swap32_add(qa[e], qa[8 + e]);
#pragma unroll
    for (int e = 0; e < 4; ++e) r4[e] = swap16_add(r8[e], r8[4 + e]);
    const bool up = lane & 8;
    const float o0 = (up ? r4[2] : r4[0]) + dpp<ROR8>(up ? r4[0] : r4[2]);
    const float o1 = (up ? r4[3] : r4[1]) + dpp<ROR8>(up ? r4[1] : r4[3]);
    *reinterpret_cast<float2*>(lds + L_QRED + xw * 512 + lane * 8) = make_float2(o0, o1);
    lds_arrive(cnt + C_QPART, lane);
  }
  if (wave == 7) {
    const u64 epoch = ((u64)(unsigned)__builtin_amdgcn_readfirstlane(ep[1]) << 32) | (unsigned)__builtin_amdgcn_readfirstlane(ep[0]);
    const u64 tg = mix64(epoch);
    const unsigned t8 = (unsigned)tg & 0xFF000000u, th = (unsigned)(tg >> 32);
    if (lane == 0) *reinterpret_cast<v4u*>(lds + L_TAG) = v4u{(unsigned)epoch, (unsigned)(epoch >> 32), t8, th};
    lds_wait(cnt + C_QPART, 4u);
    float s0 = 0.f, s1 = 0.f;
#pragma unroll
    for (int w = 0; w < 4; ++w) {
      const float2 q = *reinterpret_cast<const float2*>(lds + L_QRED + w * 512 + lane * 8);
      s0 += q.x;
      s1 += q.y;
    }
    const v4u g = {t8 | (__float_as_uint(s0) >> 8), th, t8 | (__float_as_uint(s1) >> 8), th};
    const int col = 8 * (lane & 7) + 4 * ((lane >> 4) & 1) + 2 * ((lane >> 3) & 1);
    __builtin_amdgcn_raw_buffer_store_b128(g, rws, WS_QG + (((pair * 2 + (lane >> 5)) * 2 + kh) * 1024 + qblk * 64 + col) * 8, 0, AUX_SC1);
    const unsigned t_own = uni(lds_get(cnt + C_ANCHOR));
    __builtin_amdgcn_raw_buffer_store_b64(v2u{t8 | (t_own & 0xFFFFFFu), th}, rws, lane == 0 ? WS_TIN + wg * 8 : kDrop, 0, AUX_SC1);
    lds_wait(cnt + C_RESET, 4u);
    __builtin_amdgcn_raw_buffer_store_b64(v2u{t8, th}, rws, lane == 0 ? WS_RDY + wg * 8 : kDrop, 0, AUX_SC1);
    wait_until(t_own + kTGather);
    const int qg_off = WS_QG + (pair * 2 + b) * 16384 + lane * 16;
    v4u qg[16];
    v2u tin;
    unsigned spins = 0, bad;
    do {
      MEMFENCE();
#pragma unroll
      for (int i = 0; i < 16; ++i) qg[i] = ld16<AUX_SC1>(rws, qg_off, i * 1024);
      tin = __builtin_amdgcn_raw_buffer_load_b64(rws, WS_TIN + pair * 256 + (lane & 31) * 8, 0, AUX_SC1);
      unsigned bl = tin[0] ^ t8, bh = tin[1] ^ th;
#pragma unroll
      for (int i = 0; i < 16; ++i) {
        bl |= (qg[i][0] ^ t8) | (qg[i][2] ^ t8);
        bh |= (qg[i][1] ^ th) | (qg[i][3] ^ th);
      }
      bad = (bl >> 24) | bh;
    } while (__any(bad != 0u) && ++spins < kSpinCap);
#pragma unroll
    for (int i = 0; i < 8; ++i)
      *reinterpret_cast<unsigned*>(lds + L_QIMG + i * 256 + lane * 4) =
          pack_bf16(__uint_as_float(qg[i][0] << 8) + __uint_as_float(qg[8 + i][0] << 8),
                    __uint_as_float(qg[i][2] << 8) + __uint_as_float(qg[8 + i][2] << 8));
    int lead = (int)((t_own - tin[0]) << 8) >> 8;
    lead = max(lead, __builtin_amdgcn_mov_dpp(lead, XOR1, 0xf, 0xf, true));
    lead = max(lead, __builtin_amdgcn_mov_dpp(lead, XOR2, 0xf, 0xf, true));
    lead = max(lead, __builtin_amdgcn_mov_dpp(lead, ROR4, 0xf, 0xf, true));
    lead = max(lead, __builtin_amdgcn_mov_dpp(lead, ROR8, 0xf, 0xf, true));
    lead = max(__builtin_amdgcn_readlane(lead, 0), __builtin_amdgcn_readlane(lead, 16));
    if (lane == 0) {
      lds_put(cnt + C_ANCHOR, t_own - (unsigned)min(max(lead, 0), 1000));
      lds_put(cnt + C_DUE0, t_own - (unsigned)min(max(lead, 0), 1000) + kT0 - phase);
    }
    lds_arrive(cnt + C_QIMG, lane);
  }
  PIN();
  LOAD_K(0, 0);
  LOAD_V(0, 0);
  LOAD_K(1, 0);
  LOAD_V(1, 0);
  if (!streamer || lds_get(cnt + C_QIMG) == 0u) SETTLE();
  lds_wait<1>(cnt + C_QIMG, 1u);
  const v4u tagv = *reinterpret_cast<const v4u*>(lds + L_TAG);
  const int pp = (lane >> 3) & 1, hh = lane & 7, gg = lane >> 4;
  const v4u Q0 = *reinterpret_cast<const v4u*>(lds + L_QIMG + hh * 256 + pp * 64 + gg * 16);
  const v4u Q1 = *reinterpret_cast<const v4u*>(lds + L_QIMG + hh * 256 + 128 + pp * 64 + gg * 16);

  f32x4 acc[8];
#pragma unroll
  for (int t = 0; t < 8; ++t) acc[t] = f32x4{0.f, 0.f, 0.f, 0.f};
  float m = -INFINITY, l = 0.f;
  const unsigned pmask = pp ? 0xFFFF0000u : 0x0000FFFFu;
#define TILE(buf, AFTER_QK, AFTER_PV)                                                                                \
  do {                                                                                                               \
    float s[4];                                                                                                      \
    _Pragma("unroll") for (int T = 0; T < 4; ++T) {                                                                  \
      f32x4 d = mfma16(Kt[buf][2 * T], Q0, f32x4{0.f, 0.f, 0.f, 0.f});                                               \
      d = mfma16(Kt[buf][2 * T + 1], Q1, d);                                                                         \
      s[T] = (pp ? d[3] : d[0]) + dpp<ROR8>(pp ? d[1] : d[2]);                                                       \
    }                                                                                                                \
    PIN();                                                                                                           \
    AFTER_QK;                                                                                                        \
    float mt = fmaxf(fmaxf(s[0], s[1]), fmaxf(s[2], s[3]));                                                          \
    mt = fmaxf(mt, dpp<ROR8>(mt));                                                                                   \
    mt = swap32_max(swap16_max(mt)) * kC2;                                                                           \
    const float mn = fmaxf(m, mt);                                                                                   \
    if (__any(mn != m)) {                                                                                            \
      const float alpha = __builtin_amdgcn_exp2f(m - mn);                                                            \
      l *= alpha;                                                                                                    \
      _Pragma("unroll") for (int i = 0; i < 8; ++i) acc[i] *= alpha;                                                 \
      m = mn;                                                                                                        \
    }                                                                                                                \
    v4u P;                                                                                                           \
    _Pragma("unroll") for (int T = 0; T < 4; ++T) {                                                                  \
      const float e = __builtin_amdgcn_exp2f(fmaf(s[T], kC2, -m));                                                   \
      l += e;                                                                                                        \
      P[T] = pack_bf16(e, e) & pmask;                                                                                \
    }                                                                                                                \
    _Pragma("unroll") for (int i = 0; i < 8; ++i) acc[i] = mfma16(column_of(Vt[buf], i), P, acc[i]);                 \
    PIN();                                                                                                           \
    AFTER_PV;                                                                                                        \
  } while (0)
  const int nunit = streamer ? kUnitsS : 1;
#pragma unroll 1
  for (int u = 1; u < nunit; ++u) {
    TILE(0, LOAD_K(0, u), LOAD_V(0, u));
    TILE(1, LOAD_K(1, u), LOAD_V(1, u));
  }
  v4u rdy = ld16<AUX_SC1>(rws, wave < 2 ? lane * 16 : kDrop, WS_RDY + br * 1024);
  PIN();
  if (streamer) lds_arrive(cnt + C_LAST, lane);
  PIN();
  TILE(0, PIN(), PIN());
  TILE(1, PIN(), PIN());
#undef TILE
#undef LOAD_K
#undef LOAD_V
#undef GATE

  l += dpp<ROR8>(l);
  l = swap16_add(l, l);
  l = swap32_add(l, l);
  {
    float* dst = reinterpret_cast<float*>(lds + L_MRG) + (wave * 8 + hh) * 132 + 32 * gg + 16 * pp;
#pragma unroll
    for (int rr = 0; rr < 2; ++rr)
#pragma unroll
      for (int q = 0; q < 2; ++q) {
        f32x4 o;
#pragma unroll
        for (int e = 0; e < 4; ++e) {
          const f32x4 a = acc[4 * q + e];
          o[e] = (pp ? a[2 + rr] : a[rr]) + dpp<ROR8>(pp ? a[rr] : a[2 + rr]);
        }
        *reinterpret_cast<f32x4*>(dst + 8 * rr + 4 * q) = o;
      }
    if (lane < 8) {
      float* ml = reinterpret_cast<float*>(lds + L_MRGML) + lane * 16;
      ml[wave] = m;
      ml[8 + wave] = l;
    }
  }
  SETTLE();
  lds_arrive(cnt + C_PART, lane);
  if (streamer) {
    lds_wait(cnt + C_PART, 8u);
    {
      const int hd = 2 * wave + (lane >> 5);
      const float* ml = reinterpret_cast<const float*>(lds + L_MRGML) + hd * 16;
      const f32x4 m0 = *reinterpret_cast<const f32x4*>(ml), m1 = *reinterpret_cast<const f32x4*>(ml + 4);
      const f32x4 l0 = *reinterpret_cast<const f32x4*>(ml + 8), l1 = *reinterpret_cast<const f32x4*>(ml + 12);
      f32x4 o[8];
#pragma unroll
      for (int w = 0; w < 8; ++w) o[w] = *reinterpret_cast<const f32x4*>(lds + L_MRG + ((w * 8 + hd) * 132 + 4 * (lane & 31)) * 4);
      const float M = fmaxf(fmaxf(fmaxf(m0[0], m0[1]), fmaxf(m0[2], m0[3])), fmaxf(fmaxf(m1[0], m1[1]), fmaxf(m1[2], m1[3])));
      float L = 0.f;
      f32x4 os = {0.f, 0.f, 0.f, 0.f};
#pragma unroll
      for (int w = 0; w < 8; ++w) {
        const float e = __builtin_amdgcn_exp2f((w < 4 ? m0[w & 3] : m1[w & 3]) - M);
        L = fmaf(e, w < 4 ? l0[w & 3] : l1[w & 3], L);
        os += e * o[w];
      }
      const float inv = __builtin_amdgcn_rcpf(L);
      const unsigned u0 = quiet(os[0] * inv), u1 = quiet(os[1] * inv), u2 = quiet(os[2] * inv), u3 = quiet(os[3] * inv);
      const v4u ov = {quiet(M + __builtin_amdgcn_logf(L)), __builtin_amdgcn_perm(u0, u1, 0x07060501u),
                      __builtin_amdgcn_perm(u2, u1, 0x07060302u), __builtin_amdgcn_perm(u3, u2, 0x07060501u)};
      __builtin_amdgcn_raw_buffer_store_b128(ov, rws, WS_PART + wg * kPartSlot + wave * 1024 + lane * 16, 0, AUX_SC1);
    }
    PIN();
    if (wave < 2) {
#define RDY_BAD() __any(((rdy[0] ^ tagv[2]) | (rdy[1] ^ tagv[3]) | (rdy[2] ^ tagv[2]) | (rdy[3] ^ tagv[3])) != 0u)
      if (RDY_BAD()) {
        unsigned spins = 0;
        do {
          nap(kNapPoll);
          rdy = ld16<AUX_SC1>(rws, lane * 16, WS_RDY + br * 1024);
        } while (RDY_BAD() && ++spins < kSpinCap);
      }
#undef RDY_BAD
      const int po = WS_PART + (pair * 32 + wave * 16 + (lane >> 3)) * kPartSlot + idx * 128 + 16 * (lane & 7);
      nap(kNap1);
      v4u pa = ld16<AUX_SC1>(rws, po, 0), pb = ld16<AUX_SC1>(rws, po, 8 * kPartSlot);
      for (unsigned spins = 0;; ++spins) {
        const bool ok = max(umax4(pa), umax4(pb)) != kSent;
        if (__all(ok) || spins > kSpinCap) break;
        nap(kNapPoll);
        if (!ok) {
          pa = ld16<AUX_SC1>(rws, po, 0);
          pb = ld16<AUX_SC1>(rws, po, 8 * kPartSlot);
        }
      }
      const float la = __uint_as_float(pa[0]), lb = __uint_as_float(pb[0]);
      float M = fmaxf(la, lb);
      M = fmaxf(M, dpp<ROR8>(M));
      M = swap32_max(swap16_max(M));
      const float ea = __builtin_amdgcn_exp2f(la - M), eb = __builtin_amdgcn_exp2f(lb - M);
      float v[5] = {ea + eb,
                    ea * __uint_as_float(pa[1] & 0xFFFFFF00u) + eb * __uint_as_float(pb[1] & 0xFFFFFF00u),
                    ea * __uint_as_float(__builtin_amdgcn_perm(pa[2], pa[1], 0x0504000Cu)) +
                        eb * __uint_as_float(__builtin_amdgcn_perm(pb[2], pb[1], 0x0504000Cu)),
                    ea * __uint_as_float(__builtin_amdgcn_perm(pa[2], pa[3], 0x0706000Cu)) +
                        eb * __uint_as_float(__builtin_amdgcn_perm(pb[2], pb[3], 0x0706000Cu)),
                    ea * __uint_as_float(pa[3] & 0xFFFFFF00u) + eb * __uint_as_float(pb[3] & 0xFFFFFF00u)};
#pragma unroll
      for (int i = 0; i < 5; ++i) v[i] += dpp<ROR8>(v[i]);
#pragma unroll
      for (int i = 0; i < 5; ++i) v[i] = swap16_add(v[i], v[i]);
#pragma unroll
      for (int i = 0; i < 5; ++i) v[i] = swap32_add(v[i], v[i]);
      const float inv = __builtin_amdgcn_rcpf(v[0]);
      const v2u ob = {pack_bf16(v[1] * inv, v[2] * inv), pack_bf16(v[3] * inv, v[4] * inv)};
      if (lane < 8) *reinterpret_cast<v2u*>(lds + L_OIMG + wave * 64 + lane * 8) = ob;
    }
    wg_barrier();
    if (wave != 0) return;
  } else {
    if (wave == 6) {
      const unsigned anchor = uni(lds_get(cnt + C_ANCHOR)), t8 = uni(tagv[2]), th = uni(tagv[3]);
      const unsigned t_give = anchor + kMarkAt + kMarkWait;
      while (lds_get(cnt + C_MARK2) == 0u && (int)(now_ticks() - t_give) < 0) __builtin_amdgcn_s_sleep(16);
      const unsigned m1 = lds_get(cnt + C_MARK1), m2r = lds_get(cnt + C_MARK2), m2 = m2r ? m2r : t_give;
      const unsigned span = m1 ? min(m2 - m1, 4095u) : 4095u;
      const bool tell = m2 - anchor > kMarkAt + kQuietLag || span * 100u > kMarkSpan * 103u;
      __builtin_amdgcn_raw_buffer_store_b64(v2u{t8 | (span << 12) | min(m2 - anchor, 4095u), th}, rws,
                                            lane == 0 && tell ? WS_LAG + wg * 8 : kDrop, 0, AUX_SC1);
      while ((int)(now_ticks() - (t_give + 120u)) < 0) __builtin_amdgcn_s_sleep(8);
      MEMFENCE();
      const v2u lg = __builtin_amdgcn_raw_buffer_load_b64(rws, WS_LAG + pair * 256 + (lane & 31) * 8, 0, AUX_SC1);
      const bool none = (((lg[0] ^ t8) >> 24) | (lg[1] ^ th)) != 0u;
      float sp = (float)(none ? kMarkSpan : (lg[0] >> 12) & 4095u), at = (float)(none ? kMarkAt + kUsualLag : lg[0] & 4095u);
      sp += dpp<XOR1>(sp);
      at += dpp<XOR1>(at);
      sp += dpp<XOR2>(sp);
      at += dpp<XOR2>(at);
      sp += dpp<ROR4>(sp);
      at += dpp<ROR4>(at);
      sp += dpp<ROR8>(sp);
      at += dpp<ROR8>(at);
      sp = swap16_add(sp, sp) * (1.f / (32.f * (float)(kMark2 - kMark1)));
      at = swap16_add(at, at) * (1.f / 32.f);
      const float sl = fminf(fmaxf(1.005f * sp, ((float)kTau16 + 0.4f * (at - (float)(kMarkAt + 130))) / 16.f), 32.5f);
      if (sl > 1.02f * (float)kTau16 / 16.f && lane == 0)
        line_put(cnt + C_DUE0, anchor + (unsigned)(int)(at - (float)kMark2 * sl) - 44u - phase, (unsigned)(sl * 64.f));
    }
    {
      const unsigned t_near = uni(lds_get(cnt + C_ANCHOR)) + kT0 + ((118u * kTau16) >> 4);
      while ((int)(now_ticks() - t_near) < 0) __builtin_amdgcn_s_sleep(32);
    }
    lds_wait<1>(cnt + C_LAST, 4u);
    const unsigned t_wo = now_ticks();
    PIN();
    v4u wor[16], wc[16];
#pragma unroll
    for (int i = 0; i < 16; ++i) {
      if ((i & 7) == 0) {
        unsigned tk = 0;
        if (lane == 0) tk = __hip_atomic_fetch_add(cnt + C_TICKET, 1u, __ATOMIC_RELAXED, __HIP_MEMORY_SCOPE_WORKGROUP);
        tk = uni(tk);
        const u64 ln = line_get(cnt + C_DUE0);
        const unsigned sl = uni((unsigned)(ln >> 32));
        wait_until(uni((unsigned)ln) + ((tk * sl) >> 6));
        wait_until(t_wo + (((max(tk, 128u) - 128u) * sl) >> 6));
      }
      wor[i] = ld16<AUX_NT>(ro, gg * 8 * 2048 + (lane & 15) * 16, (i & 7) * 2048 + (i >> 3) * 256 + (wave & 3) * 512);
    }
    PIN();
#pragma unroll
    for (int e = 0; e < 16; ++e) {
      wc[e] = column_of(wor + (e & 8), e & 7);
      asm volatile("" : "+v"(wc[e]));
    }
    int ba[8];
#pragma unroll
    for (int e = 0; e < 8; ++e) {
      ba[e] = ((lane & 15) >> 1) == e ? L_OIMG + (lane & 1) * 64 + gg * 16 : L_ZERO;
      asm volatile("" : "+v"(ba[e]));
    }
    wg_barrier();
    v4u bf[8];
#pragma unroll
    for (int e = 0; e < 8; ++e) bf[e] = *reinterpret_cast<const v4u*>(lds + ba[e]);
    f32x4 d0 = {0.f, 0.f, 0.f, 0.f}, d1 = {0.f, 0.f, 0.f, 0.f};
#pragma unroll
    for (int e = 0; e < 8; ++e) {
      d0 = mfma16(wc[e], bf[e], d0);
      d1 = mfma16(wc[8 + e], bf[e], d1);
    }
    const v4u o0 = {not_sentinel(d0[0]), not_sentinel(d0[1]), not_sentinel(d0[2]), not_sentinel(d0[3])};
    const v4u o1 = {not_sentinel(d1[0]), not_sentinel(d1[1]), not_sentinel(d1[2]), not_sentinel(d1[3])};
    const int so = WS_OP + wg * 8192 + (wave & 3) * 2048 + lane * 16;
    __builtin_amdgcn_raw_buffer_store_b128(o0, rws, so, 0, AUX_SC1);
    __builtin_amdgcn_raw_buffer_store_b128(o1, rws, so + 1024, 0, AUX_SC1);
    return;
  }
#undef SETTLE
#undef REPORT

  const int r = wg & 127;
  const int fo = WS_OP + (br * 128 + (lane >> 2)) * 8192 + r * 64 + (lane & 3) * 16;
  const int pos = 4 * r + (lane & 3), ln = pos & 63;
  unsigned short* dst = (unsigned short*)a10 + (ln & 1) * 2048 + br * 1024 + 128 * (pos >> 6) + 32 * (ln >> 4) + 8 * (lane >> 4) + ((ln & 15) >> 1);
  asm volatile("" : "+v"(dst));
  v4u f[8];
  nap(kNap2);
#pragma unroll
  for (int q = 0; q < 8; ++q) f[q] = ld16<AUX_SC1>(rws, fo, q * 16 * 8192);
  for (unsigned spins = 0;; ++spins) {
    unsigned mx = 0u;
#pragma unroll
    for (int q = 0; q < 8; ++q) mx = max(mx, umax4(f[q]));
    const bool ok = mx != kSent;
    if (__all(ok) || spins > kSpinCap) break;
    nap(kNapPoll);
    if (!ok) {
#pragma unroll
      for (int q = 0; q < 8; ++q) f[q] = ld16<AUX_SC1>(rws, fo, q * 16 * 8192);
    }
  }
  float y[4];
#pragma unroll
  for (int e = 0; e < 4; ++e)
    y[e] = ((__uint_as_float(f[0][e]) + __uint_as_float(f[1][e])) + (__uint_as_float(f[2][e]) + __uint_as_float(f[3][e]))) +
           ((__uint_as_float(f[4][e]) + __uint_as_float(f[5][e])) + (__uint_as_float(f[6][e]) + __uint_as_float(f[7][e])));
  const float z0 = swap32_add(y[0], y[2]), z1 = swap32_add(y[1], y[3]);
  float z = swap16_add(z0, z1);
  z += dpp<ROR4>(z);
  z += dpp<ROR8>(z);
  if ((lane & 12) == 0) *(__attribute__((address_space(1))) unsigned short*)dst = (unsigned short)(pack_bf16(z, z) & 0xFFFFu);
  if (r == 0 && lane == 0) {
    const u64 nx = (((u64)tagv[1] << 32) | tagv[0]) + 1;
    __builtin_amdgcn_raw_buffer_store_b64(v2u{(unsigned)nx, (unsigned)(nx >> 32)}, rws, WS_EPOCH + br * 256, 0, AUX_SC1);
  }
}

}

extern "C" void kernel_launch(void* const* d_in, const int* in_sizes, int n_in, void* d_out, int out_size, void* d_ws,
                              size_t ws_size, hipStream_t stream) {
  (void)in_sizes; (void)n_in; (void)out_size; (void)ws_size;
  Params p;
  for (int i = 0; i < 2; ++i) {
    p.x[i] = (const unsigned short*)d_in[5 * i + 0];
    p.k[i] = (const unsigned short*)d_in[5 * i + 1];
    p.v[i] = (const unsigned short*)d_in[5 * i + 2];
    p.wq[i] = (const unsigned short*)d_in[5 * i + 3];
    p.wo[i] = (const unsigned short*)d_in[5 * i + 4];
  }
  p.out = (unsigned short*)d_out;
  p.ws = (unsigned char*)d_ws;
  hipLaunchKernelGGL(fused_decode, dim3(kNWG), dim3(kNThr), 0, stream, p);
}
```
